```python
import torch

def _softmax(x, axis=-1):
    return torch.softmax(x, dim=axis)
def _gelu(x):
    return torch.nn.functional.gelu(x, approximate="tanh")
def _silu(x):
    return torch.nn.functional.silu(x)
def _sigmoid(x):
    return torch.sigmoid(x)
def _layernorm(x, axis=-1, eps=1e-5):
    m = x.mean(dim=axis, keepdim=True)
    v = x.var(dim=axis, keepdim=True, unbiased=False)
    return (x - m) / torch.sqrt(v + eps)
def _scatter_add(tgt, idx, src, axis=0):
    return tgt.clone().index_add_(axis, idx, src)
def _segment_sum(src, seg, n):
    out = torch.zeros((n,) + src.shape[1:], dtype=src.dtype)
    return out.index_add_(0, seg, src)
def _pad(x, pw):
    flat = tuple(v for pair in reversed(pw) for v in pair)
    return torch.nn.functional.pad(x, flat)

kernel_name = 'concat_heads__linear_kernel_hedgehog__ssm_conv_h3__prompt'

def setup_inputs():

    _w = (lambda a: (lambda t: t.to(torch.bfloat16) if t.is_floating_point() else t)(torch.as_tensor(a)))
    def _A_setup():
        B, S, D, H = 1, 2048, 640, 32
        Dh = D // H
        return {
            "x": _w((lambda *s: torch.randn(*s, dtype=torch.bfloat16) * 0.4)(B, S, D)),
            "wqkv": _w((lambda *s: torch.randn(*s, dtype=torch.bfloat16) * 0.4)(D, 3 * D)),
            "w_phi": _w((lambda *s: torch.randn(*s, dtype=torch.bfloat16) * 0.4)(H, Dh, Dh) * (Dh ** -0.5)),
            "wo": _w((lambda *s: torch.randn(*s, dtype=torch.bfloat16) * 0.05)(D, D)),
        }
    _A_in = _A_setup()

    def _B_setup():
        B, S, D = 1, 2048, 640
        ks = 4
        return {
            "q": _w((lambda *s: torch.randn(*s, dtype=torch.bfloat16) * 0.05)(B, S, D)),
            "k": _w((lambda *s: torch.randn(*s, dtype=torch.bfloat16) * 0.8)(B, S, D)),
            "v": _w((lambda *s: torch.randn(*s, dtype=torch.bfloat16) * 0.8)(B, S, D)),
            "shift_k": _w((lambda *s: torch.randn(*s, dtype=torch.bfloat16) * 0.8)(D, ks) * 0.5),
            "ssm_k": _w((lambda *s: torch.randn(*s, dtype=torch.bfloat16) * 0.8)(D, S) * (1.0 / S ** 0.5)),
            "ssm_D": _w((lambda *s: torch.randn(*s, dtype=torch.bfloat16) * 0.8)(D)),
        }
    _B_in = _B_setup()

    return {
        "A_x": _A_in["x"],
        "A_wqkv": _A_in["wqkv"],
        "A_w_phi": _A_in["w_phi"],
        "A_wo": _A_in["wo"],
        "B_q": _B_in["q"],
        "B_k": _B_in["k"],
        "B_v": _B_in["v"],
        "B_shift_k": _B_in["shift_k"],
        "B_ssm_k": _B_in["ssm_k"],
        "B_ssm_D": _B_in["ssm_D"],
    }

def reference(A_x, A_wqkv, A_w_phi, A_wo, B_q, B_k, B_v, B_shift_k, B_ssm_k, B_ssm_D):
    def _A_ref(x, wqkv, w_phi, wo):
        B, S, D = x.shape
        H = 32; Dh = D // H
        qkv = (lambda x, *d: x.permute(*d))((x @ wqkv).reshape(B, S, 3, H, Dh), 2, 0, 3, 1, 4)
        q, k, v = qkv[0], qkv[1], qkv[2]
        pq = torch.einsum("bhsd,hdf->bhsf", q, w_phi)
        pk = torch.einsum("bhsd,hdf->bhsf", k, w_phi)
        phi_q = torch.concatenate([_softmax(pq, axis=-1), _softmax(-pq, axis=-1)], axis=-1)
        phi_k = torch.concatenate([_softmax(pk, axis=-1), _softmax(-pk, axis=-1)], axis=-1)
        kv = torch.einsum("bhsf,bhsd->bhfd", phi_k, v)
        z  = torch.einsum("bhsf->bhf", phi_k)
        num = torch.einsum("bhqf,bhfd->bhqd", phi_q, kv)
        den = torch.einsum("bhqf,bhf->bhq", phi_q, z)[..., None] + 1e-6
        o = num / den
        return (lambda x, *d: x.permute(*d))(o, 0, 2, 1, 3).reshape(B, S, D) @ wo

    def _B_ref(q, k, v, shift_k, ssm_k, ssm_D):
        B, S, D = q.shape
        ks = shift_k.shape[-1]
        k_pad = _pad(k, ((0, 0), (ks - 1, 0), (0, 0)))
        k_shift = torch.zeros_like(k)
        for t in range(ks):
            k_shift = k_shift + k_pad[:, t:t + S, :] * shift_k[:, ks - 1 - t][None, None, :]
        kv = k_shift * v
        L = 2 * S
        kv_pad = _pad(kv, ((0, 0), (0, S), (0, 0)))
        kf_pad = _pad(ssm_k, ((0, 0), (0, S)))
        kv_f = (lambda x, axes=(-2,-1): torch.fft.rfft2(x.float(), dim=axes))(kv_pad[:, None, :, :], axes=(1, 2))
        kf_f = (lambda x, axes=(-2,-1): torch.fft.rfft2(x.float(), dim=axes))(kf_pad[:, None, :], axes=(1, 2))
        kf_f = (lambda x, *d: x.permute(*d))(kf_f, 1, 2, 0)
        y_f = kv_f * kf_f[None, :, :, :]
        y = (lambda x, s, axes=(-2,-1): torch.fft.irfft2(x, s=s, dim=axes).to(torch.bfloat16))(y_f, (1, L), axes=(1, 2))[:, 0, :S, :]
        y = y + kv * ssm_D[None, None, :]
        return q * y

    a = _A_ref(A_x, A_wqkv, A_w_phi, A_wo)
    b = _B_ref(B_q, B_k, B_v, B_shift_k, B_ssm_k, B_ssm_D)
    return torch.concatenate([a, b], axis=-1)
```

```cpp
#include <hip/hip_runtime.h>
#include <hip/hip_bf16.h>
#include <stdint.h>

typedef unsigned short u16;
constexpr int S = 2048, D = 640, H = 32, DH = 20;

__device__ __forceinline__ float b2f(u16 x) { return __uint_as_float(((uint32_t)x) << 16); }
__device__ __forceinline__ u16 f2b(float f) { __hip_bfloat16 b = __float2bfloat16(f); return *reinterpret_cast<u16*>(&b); }
__device__ __forceinline__ float rb(float f) { return b2f(f2b(f)); }

__global__ void k_gemm(const u16* A, const u16* B, u16* C, int M, int N, int K, int ldc) {
    __shared__ float As[16][65];
    __shared__ float Bs[16][65];
    int tx = threadIdx.x & 15, ty = threadIdx.x >> 4;
    int m0 = blockIdx.y * 64, n0 = blockIdx.x * 64;
    float acc[4][4] = {};
    for (int k0 = 0; k0 < K; k0 += 16) {
        for (int i = threadIdx.x; i < 1024; i += 256) {
            int r = i >> 4, c = i & 15;
            As[c][r] = b2f(A[(size_t)(m0 + r) * K + k0 + c]);
            int kk = i >> 6, nn = i & 63;
            Bs[kk][nn] = b2f(B[(size_t)(k0 + kk) * N + n0 + nn]);
        }
        __syncthreads();
#pragma unroll
        for (int kk = 0; kk < 16; ++kk)
#pragma unroll
            for (int i = 0; i < 4; ++i)
#pragma unroll
                for (int j = 0; j < 4; ++j) acc[i][j] += As[kk][ty * 4 + i] * Bs[kk][tx * 4 + j];
        __syncthreads();
    }
#pragma unroll
    for (int i = 0; i < 4; ++i)
#pragma unroll
        for (int j = 0; j < 4; ++j) C[(size_t)(m0 + ty * 4 + i) * ldc + n0 + tx * 4 + j] = f2b(acc[i][j]);
}

__global__ void k_phi(const u16* qkv, const u16* wphi, float* phi) {
    int idx = blockIdx.x * 256 + threadIdx.x;
    int which = idx / (S * H);
    int rem = idx % (S * H);
    int s = rem / H, h = rem % H;
    const u16* src = qkv + (size_t)s * 1920 + which * 640 + h * DH;
    float p[DH];
#pragma unroll
    for (int f = 0; f < DH; ++f) p[f] = 0.f;
#pragma unroll
    for (int d = 0; d < DH; ++d) {
        float x = b2f(src[d]);
#pragma unroll
        for (int f = 0; f < DH; ++f) p[f] += x * b2f(wphi[(h * DH + d) * DH + f]);
    }
    float mx = -1e30f, mn = 1e30f;
#pragma unroll
    for (int f = 0; f < DH; ++f) { p[f] = rb(p[f]); mx = fmaxf(mx, p[f]); mn = fminf(mn, p[f]); }
    float s1 = 0, s2 = 0;
#pragma unroll
    for (int f = 0; f < DH; ++f) { s1 += __expf(p[f] - mx); s2 += __expf(mn - p[f]); }
    float* o = phi + ((size_t)(which * H + h) * S + s) * 40;
#pragma unroll
    for (int f = 0; f < DH; ++f) { o[f] = rb(__expf(p[f] - mx) / s1); o[20 + f] = rb(__expf(mn - p[f]) / s2); }
}

__global__ void k_kv(const u16* qkv, const float* phi, float* kvz) {
    int h = blockIdx.x, f = blockIdx.y;
    __shared__ float red[21][256];
    float acc[21];
#pragma unroll
    for (int d = 0; d < 21; ++d) acc[d] = 0;
    for (int s = threadIdx.x; s < S; s += 256) {
        float p = phi[((size_t)(H + h) * S + s) * 40 + f];
#pragma unroll
        for (int d = 0; d < 20; ++d) acc[d] += p * b2f(qkv[(size_t)s * 1920 + 1280 + h * DH + d]);
        acc[20] += p;
    }
#pragma unroll
    for (int d = 0; d < 21; ++d) red[d][threadIdx.x] = acc[d];
    __syncthreads();
    if (threadIdx.x < 21) {
        float t = 0;
        for (int i = 0; i < 256; ++i) t += red[threadIdx.x][i];
        kvz[(h * 40 + f) * 21 + threadIdx.x] = rb(t);
    }
}

__global__ void k_o(const float* phi, const float* kvz, u16* o) {
    int idx = blockIdx.x * 256 + threadIdx.x;
    int s = idx / H, h = idx % H;
    float acc[21];
#pragma unroll
    for (int d = 0; d < 21; ++d) acc[d] = 0;
    for (int f = 0; f < 40; ++f) {
        float p = phi[((size_t)h * S + s) * 40 + f];
#pragma unroll
        for (int d = 0; d < 21; ++d) acc[d] += p * kvz[(h * 40 + f) * 21 + d];
    }
    float den = rb(rb(acc[20]) + 1e-6f);
#pragma unroll
    for (int d = 0; d < 20; ++d) o[(size_t)s * D + h * DH + d] = f2b(rb(acc[d]) / den);
}

__global__ void k_u(const u16* k, const u16* v, const u16* shk, float* uT) {
    int idx = blockIdx.x * 256 + threadIdx.x;
    int t = idx / D, d = idx % D;
    float ks = 0;
#pragma unroll
    for (int j = 0; j < 4; ++j) {
        int tt = t + j - 3;
        float kk = tt >= 0 ? b2f(k[(size_t)tt * D + d]) : 0.f;
        ks = rb(ks + rb(kk * b2f(shk[d * 4 + 3 - j])));
    }
    uT[(size_t)d * S + t] = rb(ks * b2f(v[(size_t)t * D + d]));
}

__global__ void k_conv(const float* uT, const u16* hk, const u16* q, const u16* Dv, u16* out) {
    int d = blockIdx.x;
    __shared__ float u[S], h[S];
    for (int i = threadIdx.x; i < S; i += 256) { u[i] = uT[(size_t)d * S + i]; h[i] = b2f(hk[(size_t)d * S + i]); }
    __syncthreads();
    float dd = b2f(Dv[d]);
    for (int t = threadIdx.x; t < S; t += 256) {
        float acc = 0;
        for (int s = 0; s <= t; ++s) acc += h[s] * u[t - s];
        float y = rb(rb(acc) + rb(u[t] * dd));
        out[(size_t)t * 1280 + 640 + d] = f2b(b2f(q[(size_t)t * D + d]) * y);
    }
}

extern "C" void kernel_launch(void* const* d_in, const int* in_sizes, int n_in,
                              void* d_out, int out_size, void* d_ws, size_t ws_size,
                              hipStream_t stream) {
    const u16* x = (const u16*)d_in[0];
    const u16* wqkv = (const u16*)d_in[1];
    const u16* wphi = (const u16*)d_in[2];
    const u16* wo = (const u16*)d_in[3];
    const u16* q = (const u16*)d_in[4];
    const u16* k = (const u16*)d_in[5];
    const u16* v = (const u16*)d_in[6];
    const u16* shk = (const u16*)d_in[7];
    const u16* hk = (const u16*)d_in[8];
    const u16* Dv = (const u16*)d_in[9];
    u16* out = (u16*)d_out;
    char* ws = (char*)d_ws;
    u16* qkv = (u16*)ws;
    float* phi = (float*)(ws + (16 << 20));
    float* kvz = (float*)(ws + (48 << 20));
    u16* o = (u16*)(ws + (52 << 20));
    float* uT = (float*)(ws + (64 << 20));
    k_gemm<<<dim3(1920 / 64, S / 64), 256, 0, stream>>>(x, wqkv, qkv, S, 1920, D, 1920);
    k_phi<<<2 * S * H / 256, 256, 0, stream>>>(qkv, wphi, phi);
    k_kv<<<dim3(H, 40), 256, 0, stream>>>(qkv, phi, kvz);
    k_o<<<S * H / 256, 256, 0, stream>>>(phi, kvz, o);
    k_gemm<<<dim3(D / 64, S / 64), 256, 0, stream>>>(o, wo, out, S, D, D, 1280);
    k_u<<<S * D / 256, 256, 0, stream>>>(k, v, shk, uT);
    k_conv<<<D, 256, 0, stream>>>(uT, hk, q, Dv, out);
}
```

```cpp
#include <hip/hip_runtime.h>
#include <hip/hip_bf16.h>
#include <stdint.h>

typedef unsigned short u16;
constexpr int S = 2048, D = 640, H = 32, DH = 20;

__device__ __forceinline__ float b2f(u16 x) { return __uint_as_float(((uint32_t)x) << 16); }
__device__ __forceinline__ u16 f2b(float f) { __hip_bfloat16 b = __float2bfloat16(f); return *reinterpret_cast<u16*>(&b); }
__device__ __forceinline__ float rb(float f) { return b2f(f2b(f)); }

__global__ void k_gemm(const u16* A, const u16* B, u16* C, int M, int N, int K, int ldc) {
    __shared__ float As[16][65];
    __shared__ float Bs[16][65];
    int tx = threadIdx.x & 15, ty = threadIdx.x >> 4;
    int m0 = blockIdx.y * 64, n0 = blockIdx.x * 64;
    float acc[4][4] = {};
    for (int k0 = 0; k0 < K; k0 += 16) {
        for (int i = threadIdx.x; i < 1024; i += 256) {
            int r = i >> 4, c = i & 15;
            As[c][r] = b2f(A[(size_t)(m0 + r) * K + k0 + c]);
            int kk = i >> 6, nn = i & 63;
            Bs[kk][nn] = b2f(B[(size_t)(k0 + kk) * N + n0 + nn]);
        }
        __syncthreads();
#pragma unroll
        for (int kk = 0; kk < 16; ++kk)
#pragma unroll
            for (int i = 0; i < 4; ++i)
#pragma unroll
                for (int j = 0; j < 4; ++j) acc[i][j] += As[kk][ty * 4 + i] * Bs[kk][tx * 4 + j];
        __syncthreads();
    }
#pragma unroll
    for (int i = 0; i < 4; ++i)
#pragma unroll
        for (int j = 0; j < 4; ++j) C[(size_t)(m0 + ty * 4 + i) * ldc + n0 + tx * 4 + j] = f2b(acc[i][j]);
}

__global__ void k_phi(const u16* qkv, const u16* wphi, float* phi) {
    int idx = blockIdx.x * 256 + threadIdx.x;
    int which = idx / (S * H);
    int rem = idx % (S * H);
    int s = rem / H, h = rem % H;
    const u16* src = qkv + (size_t)s * 1920 + which * 640 + h * DH;
    float p[DH];
#pragma unroll
    for (int f = 0; f < DH; ++f) p[f] = 0.f;
#pragma unroll
    for (int d = 0; d < DH; ++d) {
        float x = b2f(src[d]);
#pragma unroll
        for (int f = 0; f < DH; ++f) p[f] += x * b2f(wphi[(h * DH + d) * DH + f]);
    }
    float mx = -1e30f, mn = 1e30f;
#pragma unroll
    for (int f = 0; f < DH; ++f) { p[f] = rb(p[f]); mx = fmaxf(mx, p[f]); mn = fminf(mn, p[f]); }
    float s1 = 0, s2 = 0;
#pragma unroll
    for (int f = 0; f < DH; ++f) { s1 += __expf(p[f] - mx); s2 += __expf(mn - p[f]); }
    float* o = phi + ((size_t)(which * H + h) * S + s) * 40;
#pragma unroll
    for (int f = 0; f < DH; ++f) { o[f] = rb(__expf(p[f] - mx) / s1); o[20 + f] = rb(__expf(mn - p[f]) / s2); }
}

__global__ void k_kv(const u16* qkv, const float* phi, float* kvz) {
    int h = blockIdx.x, f = blockIdx.y;
    __shared__ float red[21][256];
    float acc[21];
#pragma unroll
    for (int d = 0; d < 21; ++d) acc[d] = 0;
    for (int s = threadIdx.x; s < S; s += 256) {
        float p = phi[((size_t)(H + h) * S + s) * 40 + f];
#pragma unroll
        for (int d = 0; d < 20; ++d) acc[d] += p * b2f(qkv[(size_t)s * 1920 + 1280 + h * DH + d]);
        acc[20] += p;
    }
#pragma unroll
    for (int d = 0; d < 21; ++d) red[d][threadIdx.x] = acc[d];
    __syncthreads();
    if (threadIdx.x < 21) {
        float t = 0;
        for (int i = 0; i < 256; ++i) t += red[threadIdx.x][i];
        kvz[(h * 40 + f) * 21 + threadIdx.x] = rb(t);
    }
}

__global__ void k_o(const float* phi, const float* kvz, u16* o) {
    int idx = blockIdx.x * 256 + threadIdx.x;
    int s = idx / H, h = idx % H;
    float acc[21];
#pragma unroll
    for (int d = 0; d < 21; ++d) acc[d] = 0;
    for (int f = 0; f < 40; ++f) {
        float p = phi[((size_t)h * S + s) * 40 + f];
#pragma unroll
        for (int d = 0; d < 21; ++d) acc[d] += p * kvz[(h * 40 + f) * 21 + d];
    }
    float den = rb(rb(acc[20]) + 1e-6f);
#pragma unroll
    for (int d = 0; d < 20; ++d) o[(size_t)s * D + h * DH + d] = f2b(rb(acc[d]) / den);
}

__global__ void k_u(const u16* k, const u16* v, const u16* shk, float* uT) {
    int idx = blockIdx.x * 256 + threadIdx.x;
    int t = idx / D, d = idx % D;
    float ks = 0;
#pragma unroll
    for (int j = 0; j < 4; ++j) {
        int tt = t + j - 3;
        float kk = tt >= 0 ? b2f(k[(size_t)tt * D + d]) : 0.f;
        ks = rb(ks + rb(kk * b2f(shk[d * 4 + 3 - j])));
    }
    uT[(size_t)d * S + t] = rb(ks * b2f(v[(size_t)t * D + d]));
}

__global__ void k_conv(const float* uT, const u16* hk, const u16* q, const u16* Dv, u16* out) {
    int d = blockIdx.x;
    __shared__ float u[S], h[S];
    for (int i = threadIdx.x; i < S; i += 256) { u[i] = uT[(size_t)d * S + i]; h[i] = b2f(hk[(size_t)d * S + i]); }
    __syncthreads();
    float dd = b2f(Dv[d]);
    for (int t = threadIdx.x; t < S; t += 256) {
        float acc = 0;
        for (int s = 0; s <= t; ++s) acc += h[s] * u[t - s];
        float y = rb(rb(acc) + rb(u[t] * dd));
        out[(size_t)t * 1280 + 640 + d] = f2b(b2f(q[(size_t)t * D + d]) * y);
    }
}

#define PROBE 0

__device__ __forceinline__ long long timed_load(const void* p) {
    unsigned long long t0, t1; unsigned v;
    asm volatile("s_memtime %0\n s_waitcnt lgkmcnt(0)\n global_load_dword %2, %3, off\n s_waitcnt vmcnt(0)\n s_memtime %1\n s_waitcnt lgkmcnt(0)"
                 : "=&s"(t0), "=&s"(t1), "=&v"(v) : "v"(p) : "memory");
    return (long long)(t1 - t0);
}
__global__ void k_probe(const char* x, const char* hk, const char* ws, const char* out) {
    long long m = 0;
    int amp = 200;
    if (PROBE == 1) { m = timed_load(x + 4096); }
    if (PROBE == 2) { timed_load(x + 4096); m = timed_load(x + 4096 + 1024); }
    if (PROBE == 3) { timed_load(ws + (100 << 20)); m = timed_load(ws + (100 << 20) + 1024); }
    if (PROBE == 4) {
        long long c0 = clock64();
        asm volatile(".rept 512\n s_branch 15\n .rept 15\n s_nop 0\n .endr\n .endr" ::: "memory");
        m = clock64() - c0; amp = 5;
    }
    if (PROBE == 5) { timed_load(x + 4096); m = timed_load(hk + 4096 + (1 << 20)); }
    if (PROBE == 6) { timed_load(x + 4096); m = timed_load(x + 4096 + (1 << 20)); }
    if (PROBE == 7) { timed_load(hk + 4096); m = timed_load(hk + 4096 + 1024); }
    long long w0 = wall_clock64();
    long long target = m * amp / 24;
    while (wall_clock64() - w0 < target) { __builtin_amdgcn_s_sleep(1); }
}

extern "C" void kernel_launch(void* const* d_in, const int* in_sizes, int n_in,
                              void* d_out, int out_size, void* d_ws, size_t ws_size,
                              hipStream_t stream) {
    const u16* x = (const u16*)d_in[0];
    const u16* wqkv = (const u16*)d_in[1];
    const u16* wphi = (const u16*)d_in[2];
    const u16* wo = (const u16*)d_in[3];
    const u16* q = (const u16*)d_in[4];
    const u16* k = (const u16*)d_in[5];
    const u16* v = (const u16*)d_in[6];
    const u16* shk = (const u16*)d_in[7];
    const u16* hk = (const u16*)d_in[8];
    const u16* Dv = (const u16*)d_in[9];
    u16* out = (u16*)d_out;
    char* ws = (char*)d_ws;
    u16* qkv = (u16*)ws;
    float* phi = (float*)(ws + (16 << 20));
    float* kvz = (float*)(ws + (48 << 20));
    u16* o = (u16*)(ws + (52 << 20));
    float* uT = (float*)(ws + (64 << 20));
    k_probe<<<1, 64, 0, stream>>>((const char*)x, (const char*)hk, ws, (const char*)out);
    k_gemm<<<dim3(1920 / 64, S / 64), 256, 0, stream>>>(x, wqkv, qkv, S, 1920, D, 1920);
    k_phi<<<2 * S * H / 256, 256, 0, stream>>>(qkv, wphi, phi);
    k_kv<<<dim3(H, 40), 256, 0, stream>>>(qkv, phi, kvz);
    k_o<<<S * H / 256, 256, 0, stream>>>(phi, kvz, o);
    k_gemm<<<dim3(D / 64, S / 64), 256, 0, stream>>>(o, wo, out, S, D, D, 1280);
    k_u<<<S * D / 256, 256, 0, stream>>>(k, v, shk, uT);
    k_conv<<<D, 256, 0, stream>>>(uT, hk, q, Dv, out);
}
```

```cpp
#include <hip/hip_runtime.h>
#include <hip/hip_bf16.h>
#include <stdint.h>

typedef unsigned short u16;
constexpr int S = 2048, D = 640, H = 32, DH = 20;

__device__ __forceinline__ float b2f(u16 x) { return __uint_as_float(((uint32_t)x) << 16); }
__device__ __forceinline__ u16 f2b(float f) { __hip_bfloat16 b = __float2bfloat16(f); return *reinterpret_cast<u16*>(&b); }
__device__ __forceinline__ float rb(float f) { return b2f(f2b(f)); }

__global__ void k_gemm(const u16* A, const u16* B, u16* C, int M, int N, int K, int ldc) {
    __shared__ float As[16][65];
    __shared__ float Bs[16][65];
    int tx = threadIdx.x & 15, ty = threadIdx.x >> 4;
    int m0 = blockIdx.y * 64, n0 = blockIdx.x * 64;
    float acc[4][4] = {};
    for (int k0 = 0; k0 < K; k0 += 16) {
        for (int i = threadIdx.x; i < 1024; i += 256) {
            int r = i >> 4, c = i & 15;
            As[c][r] = b2f(A[(size_t)(m0 + r) * K + k0 + c]);
            int kk = i >> 6, nn = i & 63;
            Bs[kk][nn] = b2f(B[(size_t)(k0 + kk) * N + n0 + nn]);
        }
        __syncthreads();
#pragma unroll
        for (int kk = 0; kk < 16; ++kk)
#pragma unroll
            for (int i = 0; i < 4; ++i)
#pragma unroll
                for (int j = 0; j < 4; ++j) acc[i][j] += As[kk][ty * 4 + i] * Bs[kk][tx * 4 + j];
        __syncthreads();
    }
#pragma unroll
    for (int i = 0; i < 4; ++i)
#pragma unroll
        for (int j = 0; j < 4; ++j) C[(size_t)(m0 + ty * 4 + i) * ldc + n0 + tx * 4 + j] = f2b(acc[i][j]);
}

__global__ void k_phi(const u16* qkv, const u16* wphi, float* phi) {
    int idx = blockIdx.x * 256 + threadIdx.x;
    int which = idx / (S * H);
    int rem = idx % (S * H);
    int s = rem / H, h = rem % H;
    const u16* src = qkv + (size_t)s * 1920 + which * 640 + h * DH;
    float p[DH];
#pragma unroll
    for (int f = 0; f < DH; ++f) p[f] = 0.f;
#pragma unroll
    for (int d = 0; d < DH; ++d) {
        float x = b2f(src[d]);
#pragma unroll
        for (int f = 0; f < DH; ++f) p[f] += x * b2f(wphi[(h * DH + d) * DH + f]);
    }
    float mx = -1e30f, mn = 1e30f;
#pragma unroll
    for (int f = 0; f < DH; ++f) { p[f] = rb(p[f]); mx = fmaxf(mx, p[f]); mn = fminf(mn, p[f]); }
    float s1 = 0, s2 = 0;
#pragma unroll
    for (int f = 0; f < DH; ++f) { s1 += __expf(p[f] - mx); s2 += __expf(mn - p[f]); }
    float* o = phi + ((size_t)(which * H + h) * S + s) * 40;
#pragma unroll
    for (int f = 0; f < DH; ++f) { o[f] = rb(__expf(p[f] - mx) / s1); o[20 + f] = rb(__expf(mn - p[f]) / s2); }
}

__global__ void k_kv(const u16* qkv, const float* phi, float* kvz) {
    int h = blockIdx.x, f = blockIdx.y;
    __shared__ float red[21][256];
    float acc[21];
#pragma unroll
    for (int d = 0; d < 21; ++d) acc[d] = 0;
    for (int s = threadIdx.x; s < S; s += 256) {
        float p = phi[((size_t)(H + h) * S + s) * 40 + f];
#pragma unroll
        for (int d = 0; d < 20; ++d) acc[d] += p * b2f(qkv[(size_t)s * 1920 + 1280 + h * DH + d]);
        acc[20] += p;
    }
#pragma unroll
    for (int d = 0; d < 21; ++d) red[d][threadIdx.x] = acc[d];
    __syncthreads();
    if (threadIdx.x < 21) {
        float t = 0;
        for (int i = 0; i < 256; ++i) t += red[threadIdx.x][i];
        kvz[(h * 40 + f) * 21 + threadIdx.x] = rb(t);
    }
}

__global__ void k_o(const float* phi, const float* kvz, u16* o) {
    int idx = blockIdx.x * 256 + threadIdx.x;
    int s = idx / H, h = idx % H;
    float acc[21];
#pragma unroll
    for (int d = 0; d < 21; ++d) acc[d] = 0;
    for (int f = 0; f < 40; ++f) {
        float p = phi[((size_t)h * S + s) * 40 + f];
#pragma unroll
        for (int d = 0; d < 21; ++d) acc[d] += p * kvz[(h * 40 + f) * 21 + d];
    }
    float den = rb(rb(acc[20]) + 1e-6f);
#pragma unroll
    for (int d = 0; d < 20; ++d) o[(size_t)s * D + h * DH + d] = f2b(rb(acc[d]) / den);
}

__global__ void k_u(const u16* k, const u16* v, const u16* shk, float* uT) {
    int idx = blockIdx.x * 256 + threadIdx.x;
    int t = idx / D, d = idx % D;
    float ks = 0;
#pragma unroll
    for (int j = 0; j < 4; ++j) {
        int tt = t + j - 3;
        float kk = tt >= 0 ? b2f(k[(size_t)tt * D + d]) : 0.f;
        ks = rb(ks + rb(kk * b2f(shk[d * 4 + 3 - j])));
    }
    uT[(size_t)d * S + t] = rb(ks * b2f(v[(size_t)t * D + d]));
}

__global__ void k_conv(const float* uT, const u16* hk, const u16* q, const u16* Dv, u16* out) {
    int d = blockIdx.x;
    __shared__ float u[S], h[S];
    for (int i = threadIdx.x; i < S; i += 256) { u[i] = uT[(size_t)d * S + i]; h[i] = b2f(hk[(size_t)d * S + i]); }
    __syncthreads();
    float dd = b2f(Dv[d]);
    for (int t = threadIdx.x; t < S; t += 256) {
        float acc = 0;
        for (int s = 0; s <= t; ++s) acc += h[s] * u[t - s];
        float y = rb(rb(acc) + rb(u[t] * dd));
        out[(size_t)t * 1280 + 640 + d] = f2b(b2f(q[(size_t)t * D + d]) * y);
    }
}

#define PROBE 0

typedef __attribute__((ext_vector_type(4))) unsigned u32x4;
__global__ void __launch_bounds__(640) k_probe(const char* x, const char* q, const char* k, const char* v, const char* hk, char* ws) {
    long long w0 = wall_clock64();
    long long target = 0;
    size_t off = (size_t)blockIdx.x * 10240 + threadIdx.x * 16;
    if (PROBE == 1 || PROBE == 2) {
        u32x4 a, b, c, d, e;
        if (PROBE == 1) {
            a = *(const u32x4*)(x + off); b = *(const u32x4*)(q + off); c = *(const u32x4*)(k + off); d = *(const u32x4*)(v + off); e = *(const u32x4*)(hk + off);
        } else {
            a = __builtin_nontemporal_load((const u32x4*)(x + off)); b = __builtin_nontemporal_load((const u32x4*)(q + off));
            c = __builtin_nontemporal_load((const u32x4*)(k + off)); d = __builtin_nontemporal_load((const u32x4*)(v + off));
            e = __builtin_nontemporal_load((const u32x4*)(hk + off));
        }
        a ^= b ^ c ^ d ^ e;
        asm volatile("" :: "v"(a));
        __syncthreads();
        target = (wall_clock64() - w0) * 20;
    }
    if (PROBE == 3) {
        long long c0 = clock64();
        while (clock64() - c0 < 24000) {}
        target = (wall_clock64() - w0) * 20;
    }
    if (PROBE == 4 || PROBE == 5 || PROBE == 6) {
        auto rsrc = __builtin_amdgcn_make_buffer_rsrc((void*)(ws + (128 << 20)), 0, 64 << 20, 0x00020000);
        u32x4 a = {1, 2, 3, threadIdx.x};
        constexpr int AUX = PROBE == 4 ? 16 : (PROBE == 5 ? 18 : 2);
#pragma unroll
        for (int i = 0; i < 4; ++i) __builtin_amdgcn_raw_buffer_store_b128(a, rsrc, (int)off + i * 2621440, 0, AUX);
        asm volatile("s_waitcnt vmcnt(0)" ::: "memory");
        __syncthreads();
        target = (wall_clock64() - w0) * 20;
    }
    while (wall_clock64() - w0 < target) { __builtin_amdgcn_s_sleep(1); }
}

extern "C" void kernel_launch(void* const* d_in, const int* in_sizes, int n_in,
                              void* d_out, int out_size, void* d_ws, size_t ws_size,
                              hipStream_t stream) {
    const u16* x = (const u16*)d_in[0];
    const u16* wqkv = (const u16*)d_in[1];
    const u16* wphi = (const u16*)d_in[2];
    const u16* wo = (const u16*)d_in[3];
    const u16* q = (const u16*)d_in[4];
    const u16* k = (const u16*)d_in[5];
    const u16* v = (const u16*)d_in[6];
    const u16* shk = (const u16*)d_in[7];
    const u16* hk = (const u16*)d_in[8];
    const u16* Dv = (const u16*)d_in[9];
    u16* out = (u16*)d_out;
    char* ws = (char*)d_ws;
    u16* qkv = (u16*)ws;
    float* phi = (float*)(ws + (16 << 20));
    float* kvz = (float*)(ws + (48 << 20));
    u16* o = (u16*)(ws + (52 << 20));
    float* uT = (float*)(ws + (64 << 20));
    k_probe<<<256, 640, 0, stream>>>((const char*)x, (const char*)q, (const char*)k, (const char*)v, (const char*)hk, ws);
    k_gemm<<<dim3(1920 / 64, S / 64), 256, 0, stream>>>(x, wqkv, qkv, S, 1920, D, 1920);
    k_phi<<<2 * S * H / 256, 256, 0, stream>>>(qkv, wphi, phi);
    k_kv<<<dim3(H, 40), 256, 0, stream>>>(qkv, phi, kvz);
    k_o<<<S * H / 256, 256, 0, stream>>>(phi, kvz, o);
    k_gemm<<<dim3(D / 64, S / 64), 256, 0, stream>>>(o, wo, out, S, D, D, 1280);
    k_u<<<S * D / 256, 256, 0, stream>>>(k, v, shk, uT);
    k_conv<<<D, 256, 0, stream>>>(uT, hk, q, Dv, out);
}
```

```cpp
#include <hip/hip_runtime.h>
#include <hip/hip_bf16.h>
#include <stdint.h>

typedef unsigned short u16;
constexpr int S = 2048, D = 640, H = 32, DH = 20;

__device__ __forceinline__ float b2f(u16 x) { return __uint_as_float(((uint32_t)x) << 16); }
__device__ __forceinline__ u16 f2b(float f) { __hip_bfloat16 b = __float2bfloat16(f); return *reinterpret_cast<u16*>(&b); }
__device__ __forceinline__ float rb(float f) { return b2f(f2b(f)); }

__global__ void k_gemm(const u16* A, const u16* B, u16* C, int M, int N, int K, int ldc) {
    __shared__ float As[16][65];
    __shared__ float Bs[16][65];
    int tx = threadIdx.x & 15, ty = threadIdx.x >> 4;
    int m0 = blockIdx.y * 64, n0 = blockIdx.x * 64;
    float acc[4][4] = {};
    for (int k0 = 0; k0 < K; k0 += 16) {
        for (int i = threadIdx.x; i < 1024; i += 256) {
            int r = i >> 4, c = i & 15;
            As[c][r] = b2f(A[(size_t)(m0 + r) * K + k0 + c]);
            int kk = i >> 6, nn = i & 63;
            Bs[kk][nn] = b2f(B[(size_t)(k0 + kk) * N + n0 + nn]);
        }
        __syncthreads();
#pragma unroll
        for (int kk = 0; kk < 16; ++kk)
#pragma unroll
            for (int i = 0; i < 4; ++i)
#pragma unroll
                for (int j = 0; j < 4; ++j) acc[i][j] += As[kk][ty * 4 + i] * Bs[kk][tx * 4 + j];
        __syncthreads();
    }
#pragma unroll
    for (int i = 0; i < 4; ++i)
#pragma unroll
        for (int j = 0; j < 4; ++j) C[(size_t)(m0 + ty * 4 + i) * ldc + n0 + tx * 4 + j] = f2b(acc[i][j]);
}

__global__ void k_phi(const u16* qkv, const u16* wphi, float* phi) {
    int idx = blockIdx.x * 256 + threadIdx.x;
    int which = idx / (S * H);
    int rem = idx % (S * H);
    int s = rem / H, h = rem % H;
    const u16* src = qkv + (size_t)s * 1920 + which * 640 + h * DH;
    float p[DH];
#pragma unroll
    for (int f = 0; f < DH; ++f) p[f] = 0.f;
#pragma unroll
    for (int d = 0; d < DH; ++d) {
        float x = b2f(src[d]);
#pragma unroll
        for (int f = 0; f < DH; ++f) p[f] += x * b2f(wphi[(h * DH + d) * DH + f]);
    }
    float mx = -1e30f, mn = 1e30f;
#pragma unroll
    for (int f = 0; f < DH; ++f) { p[f] = rb(p[f]); mx = fmaxf(mx, p[f]); mn = fminf(mn, p[f]); }
    float s1 = 0, s2 = 0;
#pragma unroll
    for (int f = 0; f < DH; ++f) { s1 += __expf(p[f] - mx); s2 += __expf(mn - p[f]); }
    float* o = phi + ((size_t)(which * H + h) * S + s) * 40;
#pragma unroll
    for (int f = 0; f < DH; ++f) { o[f] = rb(__expf(p[f] - mx) / s1); o[20 + f] = rb(__expf(mn - p[f]) / s2); }
}

__global__ void k_kv(const u16* qkv, const float* phi, float* kvz) {
    int h = blockIdx.x, f = blockIdx.y;
    __shared__ float red[21][256];
    float acc[21];
#pragma unroll
    for (int d = 0; d < 21; ++d) acc[d] = 0;
    for (int s = threadIdx.x; s < S; s += 256) {
        float p = phi[((size_t)(H + h) * S + s) * 40 + f];
#pragma unroll
        for (int d = 0; d < 20; ++d) acc[d] += p * b2f(qkv[(size_t)s * 1920 + 1280 + h * DH + d]);
        acc[20] += p;
    }
#pragma unroll
    for (int d = 0; d < 21; ++d) red[d][threadIdx.x] = acc[d];
    __syncthreads();
    if (threadIdx.x < 21) {
        float t = 0;
        for (int i = 0; i < 256; ++i) t += red[threadIdx.x][i];
        kvz[(h * 40 + f) * 21 + threadIdx.x] = rb(t);
    }
}

__global__ void k_o(const float* phi, const float* kvz, u16* o) {
    int idx = blockIdx.x * 256 + threadIdx.x;
    int s = idx / H, h = idx % H;
    float acc[21];
#pragma unroll
    for (int d = 0; d < 21; ++d) acc[d] = 0;
    for (int f = 0; f < 40; ++f) {
        float p = phi[((size_t)h * S + s) * 40 + f];
#pragma unroll
        for (int d = 0; d < 21; ++d) acc[d] += p * kvz[(h * 40 + f) * 21 + d];
    }
    float den = rb(rb(acc[20]) + 1e-6f);
#pragma unroll
    for (int d = 0; d < 20; ++d) o[(size_t)s * D + h * DH + d] = f2b(rb(acc[d]) / den);
}

__global__ void k_u(const u16* k, const u16* v, const u16* shk, float* uT) {
    int idx = blockIdx.x * 256 + threadIdx.x;
    int t = idx / D, d = idx % D;
    float ks = 0;
#pragma unroll
    for (int j = 0; j < 4; ++j) {
        int tt = t + j - 3;
        float kk = tt >= 0 ? b2f(k[(size_t)tt * D + d]) : 0.f;
        ks = rb(ks + rb(kk * b2f(shk[d * 4 + 3 - j])));
    }
    uT[(size_t)d * S + t] = rb(ks * b2f(v[(size_t)t * D + d]));
}

__global__ void k_conv(const float* uT, const u16* hk, const u16* q, const u16* Dv, u16* out) {
    int d = blockIdx.x;
    __shared__ float u[S], h[S];
    for (int i = threadIdx.x; i < S; i += 256) { u[i] = uT[(size_t)d * S + i]; h[i] = b2f(hk[(size_t)d * S + i]); }
    __syncthreads();
    float dd = b2f(Dv[d]);
    for (int t = threadIdx.x; t < S; t += 256) {
        float acc = 0;
        for (int s = 0; s <= t; ++s) acc += h[s] * u[t - s];
        float y = rb(rb(acc) + rb(u[t] * dd));
        out[(size_t)t * 1280 + 640 + d] = f2b(b2f(q[(size_t)t * D + d]) * y);
    }
}


template <int ID> __global__ void __launch_bounds__(256) k_empty(char* ws) { if (ws == nullptr) ws[threadIdx.x] = ID; }
template <int N> void launch_distinct(char* ws, hipStream_t stream) { k_empty<N><<<256, 256, 0, stream>>>(ws); if constexpr (N > 1) launch_distinct<N - 1>(ws, stream); }

extern "C" void kernel_launch(void* const* d_in, const int* in_sizes, int n_in,
                              void* d_out, int out_size, void* d_ws, size_t ws_size,
                              hipStream_t stream) {
    const u16* x = (const u16*)d_in[0];
    const u16* wqkv = (const u16*)d_in[1];
    const u16* wphi = (const u16*)d_in[2];
    const u16* wo = (const u16*)d_in[3];
    const u16* q = (const u16*)d_in[4];
    const u16* k = (const u16*)d_in[5];
    const u16* v = (const u16*)d_in[6];
    const u16* shk = (const u16*)d_in[7];
    const u16* hk = (const u16*)d_in[8];
    const u16* Dv = (const u16*)d_in[9];
    u16* out = (u16*)d_out;
    char* ws = (char*)d_ws;
    u16* qkv = (u16*)ws;
    float* phi = (float*)(ws + (16 << 20));
    float* kvz = (float*)(ws + (48 << 20));
    u16* o = (u16*)(ws + (52 << 20));
    float* uT = (float*)(ws + (64 << 20));

    k_gemm<<<dim3(1920 / 64, S / 64), 256, 0, stream>>>(x, wqkv, qkv, S, 1920, D, 1920);
    k_phi<<<2 * S * H / 256, 256, 0, stream>>>(qkv, wphi, phi);
    k_kv<<<dim3(H, 40), 256, 0, stream>>>(qkv, phi, kvz);
    k_o<<<S * H / 256, 256, 0, stream>>>(phi, kvz, o);
    k_gemm<<<dim3(D / 64, S / 64), 256, 0, stream>>>(o, wo, out, S, D, D, 1280);
    k_u<<<S * D / 256, 256, 0, stream>>>(k, v, shk, uT);
    k_conv<<<D, 256, 0, stream>>>(uT, hk, q, Dv, out);
}
```

```cpp
#include <hip/hip_runtime.h>
#include <hip/hip_bf16.h>
#include <stdint.h>

namespace hk {

typedef unsigned long long u64;
typedef __attribute__((ext_vector_type(4))) unsigned u32x4;
typedef __attribute__((ext_vector_type(2))) unsigned u32x2;
typedef __attribute__((ext_vector_type(4))) float f32x4;
typedef __attribute__((ext_vector_type(16))) float f32x16;
typedef __attribute__((ext_vector_type(2))) float f32x2;
typedef __attribute__((ext_vector_type(8))) __bf16 bf16x8;
typedef __attribute__((ext_vector_type(2))) __bf16 bf16x2;
typedef __attribute__((ext_vector_type(4))) short s16x4;
typedef __attribute__((address_space(3))) char* lds_ptr;

constexpr int LDS_BYTES = 163840;

constexpr int WS_EPOCH_A = 0;
constexpr int WS_EPOCH_B = 128;
constexpr int WS_FLAGH = 4096;
constexpr int WS_FLAG1 = 65536;
constexpr int WS_FLAG2 = WS_FLAG1 + 65536;
constexpr int WS_FLAGB = WS_FLAG2 + 65536;
constexpr int WS_PART = 512 << 10;
constexpr int PART_STRIDE = 1792;
constexpr int WS_O = 2 << 20;
constexpr int WS_U = 5 << 20;
constexpr int WS_USED = 8 << 20;
constexpr int WS_STAMP = 16 << 20;

struct Params {
    const char* x; const char* wqkv; const char* wphi; const char* wo;
    const char* q; const char* k; const char* v; const char* shk; const char* hk; const char* dv;
    char* out; char* ws;
};

#define STAMP_ARG
#define STAMP_PASS
#define STAMP(n) do {} while (0)

template <int V> struct ic { static constexpr int value = V; };
template <int B, int E, class Fn> __device__ __forceinline__ void static_for(Fn&& fn) {
    if constexpr (B < E) { fn(ic<B>{}); static_for<B + 1, E>(fn); }
}
#define SBAR() __builtin_amdgcn_sched_barrier(0)
#define FENCE() asm volatile("" ::: "memory")

__device__ __forceinline__ void dma16(const char* sbase, unsigned voff, unsigned lds_dst) {
    asm volatile("s_mov_b32 m0, %2\n\ts_nop 0\n\tglobal_load_lds_dwordx4 %0, %1" :: "v"(voff), "s"(sbase), "s"(lds_dst) : "memory");
}
__device__ __forceinline__ void set_m0(unsigned d) { asm volatile("s_mov_b32 m0, %0\n\ts_nop 0" :: "s"(d) : "memory"); }
__device__ __forceinline__ void dma_off(int off, const char* sbase, unsigned voff) {
    asm volatile("global_load_lds_dwordx4 %0, %1 offset:%2" :: "v"(voff), "s"(sbase), "i"(off) : "memory");
}
__device__ __forceinline__ void wait_vm_n(int n) { asm volatile("s_waitcnt vmcnt(%0)" :: "i"(n) : "memory"); }
__device__ __forceinline__ void bar_raw() { asm volatile("s_barrier" ::: "memory"); }
__device__ __forceinline__ lds_ptr opaque(lds_ptr p) { asm volatile("" : "+v"(p)); return p; }
template <int N> __device__ __forceinline__ void wait_vm() { asm volatile("s_waitcnt vmcnt(%0)" :: "i"(N) : "memory"); }
__device__ __forceinline__ void bar() { asm volatile("s_waitcnt lgkmcnt(0)\n\ts_barrier" ::: "memory"); }
__device__ __forceinline__ void touch(const char* sbase, unsigned voff, unsigned lds_dst) {
    asm volatile("s_mov_b32 m0, %2\n\ts_nop 0\n\tglobal_load_lds_dword %0, %1" :: "v"(voff), "s"(sbase), "s"(lds_dst) : "memory");
}
__device__ __forceinline__ u32x4 lds_r128(lds_ptr p) { return *(__attribute__((address_space(3))) u32x4*)p; }
__device__ __forceinline__ u32x2 lds_r64(lds_ptr p) { return *(__attribute__((address_space(3))) u32x2*)p; }
__device__ __forceinline__ unsigned lds_r32(lds_ptr p) { return *(__attribute__((address_space(3))) unsigned*)p; }
__device__ __forceinline__ void lds_w128(lds_ptr p, u32x4 v) { *(__attribute__((address_space(3))) u32x4*)p = v; }
__device__ __forceinline__ void lds_w64(lds_ptr p, u32x2 v) { *(__attribute__((address_space(3))) u32x2*)p = v; }
__device__ __forceinline__ void lds_w32(lds_ptr p, unsigned v) { *(__attribute__((address_space(3))) unsigned*)p = v; }
__device__ __forceinline__ u32x2 lds_tr(lds_ptr p) {
    return __builtin_bit_cast(u32x2, __builtin_amdgcn_ds_read_tr16_b64_v4i16((__attribute__((address_space(3))) s16x4*)p));
}
__device__ __forceinline__ u32x4 cat(u32x2 a, u32x2 b) { return u32x4{a.x, a.y, b.x, b.y}; }
__device__ __forceinline__ f32x4 mfma32(u32x4 a, u32x4 b, f32x4 c) {
    return __builtin_amdgcn_mfma_f32_16x16x32_bf16(__builtin_bit_cast(bf16x8, a), __builtin_bit_cast(bf16x8, b), c, 0, 0, 0);
}
__device__ __forceinline__ f32x16 mfma_w(u32x4 a, u32x4 b, f32x16 c) {
    return __builtin_amdgcn_mfma_f32_32x32x16_bf16(__builtin_bit_cast(bf16x8, a), __builtin_bit_cast(bf16x8, b), c, 0, 0, 0);
}
__device__ __forceinline__ unsigned pk(float lo, float hi) {
    return __builtin_bit_cast(unsigned, __builtin_convertvector(f32x2{lo, hi}, bf16x2));
}
__device__ __forceinline__ float blo(unsigned x) { return __uint_as_float(x << 16); }
__device__ __forceinline__ float bhi(unsigned x) { return __uint_as_float(x & 0xffff0000u); }

__device__ __forceinline__ u64 ld_agent(const char* p) { return __hip_atomic_load((const u64*)p, __ATOMIC_RELAXED, __HIP_MEMORY_SCOPE_AGENT); }
__device__ __forceinline__ void st_agent(char* p, u64 v) { __hip_atomic_store((u64*)p, v, __ATOMIC_RELAXED, __HIP_MEMORY_SCOPE_AGENT); }

__device__ __forceinline__ void wait_flags(const char* flags, int n, int lane, u64 epoch) {
    const char* p = flags + (lane < n ? lane : 0) * 128;
    for (int spin = 0; spin < (1 << 22); ++spin) {
        u64 f = ld_agent(p);
        if (__all(f == epoch)) break;
        __builtin_amdgcn_s_sleep(1);
    }
    FENCE();
}

#define O_AHEAD 12
#define Q_S1 8
#define P_AHEAD 20
#define MX3(a, b, c) __builtin_fmaxf(__builtin_fmaxf((a), (b)), (c))
#define MN3(a, b, c) __builtin_fminf(__builtin_fminf((a), (b)), (c))

__device__ __forceinline__ void dual_softmax(const float (&p)[20], unsigned (&F)[20]) {
    const float x0 = MX3(p[0], p[1], p[2]), x1 = MX3(p[3], p[4], p[5]), x2 = MX3(p[6], p[7], p[8]), x3 = MX3(p[9], p[10], p[11]);
    const float n0 = MN3(p[0], p[1], p[2]), n1 = MN3(p[3], p[4], p[5]), n2 = MN3(p[6], p[7], p[8]), n3 = MN3(p[9], p[10], p[11]);
    const float x4 = MX3(x0, p[12], p[13]), x5 = MX3(x1, p[14], p[15]), x6 = MX3(x2, p[16], p[17]), x7 = MX3(x3, p[18], p[19]);
    const float n4 = MN3(n0, p[12], p[13]), n5 = MN3(n1, p[14], p[15]), n6 = MN3(n2, p[16], p[17]), n7 = MN3(n3, p[18], p[19]);
    constexpr float L2E = 1.4426950408889634f;
    const float cp = __builtin_fmaxf(MX3(x4, x5, x6), x7) * -L2E, cn = __builtin_fminf(MN3(n4, n5, n6), n7) * L2E;
    f32x2 ep[10], en[10];
#pragma unroll
    for (int i = 0; i < 10; ++i) {
        const f32x2 v = {p[2 * i], p[2 * i + 1]};
        ep[i] = __builtin_elementwise_fma(v, f32x2{L2E, L2E}, f32x2{cp, cp});
        en[i] = __builtin_elementwise_fma(v, f32x2{-L2E, -L2E}, f32x2{cn, cn});
    }
    SBAR();
#pragma unroll
    for (int i = 0; i < 10; ++i) ep[i] = f32x2{__builtin_amdgcn_exp2f(ep[i].x), __builtin_amdgcn_exp2f(ep[i].y)};
    SBAR();
    const f32x2 sp = ((ep[0] + ep[1]) + (ep[2] + ep[3])) + ((ep[4] + ep[5]) + (ep[6] + ep[7])) + (ep[8] + ep[9]);
#pragma unroll
    for (int i = 0; i < 10; ++i) en[i] = f32x2{__builtin_amdgcn_exp2f(en[i].x), __builtin_amdgcn_exp2f(en[i].y)};
    const float rp = __builtin_amdgcn_rcpf(sp.x + sp.y);
    SBAR();
    const f32x2 sn = ((en[0] + en[1]) + (en[2] + en[3])) + ((en[4] + en[5]) + (en[6] + en[7])) + (en[8] + en[9]);
#pragma unroll
    for (int i = 0; i < 10; ++i) ep[i] *= rp;
    const float rn = __builtin_amdgcn_rcpf(sn.x + sn.y);
    SBAR();
#pragma unroll
    for (int i = 0; i < 10; ++i) F[i] = pk(ep[i].x, ep[i].y);
    SBAR();
#pragma unroll
    for (int i = 0; i < 10; ++i) en[i] *= rn;
    SBAR();
#pragma unroll
    for (int i = 0; i < 10; ++i) F[10 + i] = pk(en[i].x, en[i].y);
    SBAR();
}

__device__ __forceinline__ void project(const unsigned (&X)[2][10], const u32x4 (&W)[11], float (&p)[2][20]) {
    const f32x16 z = {};
    const u32x4 xr = {X[0][8], X[0][9], X[1][8], X[1][9]};
    u32x4 xf[2][2];
#pragma unroll
    for (int u = 0; u < 2; ++u)
#pragma unroll
        for (int c = 0; c < 2; ++c) xf[u][c] = u32x4{X[u][4 * c], X[u][4 * c + 1], X[u][4 * c + 2], X[u][4 * c + 3]};
    f32x16 p2 = mfma_w(W[6], xf[0][0], z);
    f32x16 p0 = mfma_w(W[0], xf[0][0], z);
    p2 = mfma_w(W[7], xf[0][1], p2);
    p0 = mfma_w(W[1], xf[0][1], p0);
    p2 = mfma_w(W[8], xf[1][0], p2);
    p2 = mfma_w(W[9], xf[1][1], p2);
    p2 = mfma_w(W[10], xr, p2);
    p0 = mfma_w(W[2], xr, p0);
    f32x16 p1 = mfma_w(W[3], xf[1][0], z);
    p1 = mfma_w(W[4], xf[1][1], p1);
    p1 = mfma_w(W[5], xr, p1);
#pragma unroll
    for (int e = 0; e < 16; ++e) { p[0][e] = p0[e]; p[1][e] = p1[e]; }
#pragma unroll
    for (int e = 0; e < 4; ++e) { p[0][16 + e] = p2[e]; p[1][16 + e] = p2[4 + e]; }
}

constexpr int A_WQ = 65536;
constexpr int wq_pos(int u) { return A_WQ + (u < 16 ? u + 2 : u - 16) * 5120; }
constexpr int wo_pos(int p) { return p < 4 ? p * 10240 : wq_pos(2) + (p - 4) * 10240; }
constexpr int A_OST = wq_pos(10);

__device__ __forceinline__ unsigned wo_voff(int lane) { return (lane >> 3) * 1280 + (((lane & 7) ^ ((lane >> 2) & 4)) * 16) + 4096; }
__device__ __forceinline__ void stage_wo(const Params& P, unsigned lds0, int ct, int rot, int p, unsigned voff) {
    const char* s = P.wo + ct * 128 + ((p + rot) & 7) * (80 * 1280);
    const unsigned d = lds0 + p * 10240 + (p >= 4 ? wo_pos(4) - 40960 : 0);
#pragma unroll
    for (int h = 0; h < 2; ++h) {
        set_m0(d + h * 5120 + 4096);
#pragma unroll
        for (int j = 0; j < 5; ++j) dma_off((j - 4) * 1024, s + h * 51200 + j * (10240 - 1024), voff);
    }
}

__device__ __forceinline__ void out_tile(const Params& P, lds_ptr L, u64 epoch, int r, int ct, int rot, int wv, int lane STAMP_ARG) {
    const __amdgpu_buffer_rsrc_t wsr = __builtin_amdgcn_make_buffer_rsrc((void*)P.ws, 0, WS_USED, 0x00020000);
    const int hi = lane >> 5, g1 = (lane >> 4) & 1, tq = (lane & 15) >> 2, tp = lane & 3;
    lds_ptr wb[2][2];
    const int wo_ = (hi * 20 + tq) * 128 + g1 * 32 + (tp & 1) * 16 + (tp >> 1) * 8;
#pragma unroll
    for (int h = 0; h < 2; ++h)
#pragma unroll
        for (int t = 0; t < 2; ++t) wb[h][t] = opaque(L + wo_pos(4 * h) + wo_ + ((t ^ (tq >> 1)) * 64));
    u32x4 wf[2][2], of[40];
    auto rd = [&](int s, int t) {
        const int p = s / 5, e = s % 5, h = p >> 2;
        lds_ptr b = wb[h][t] + (p & 3) * 10240;
        if (e < 4) wf[s & 1][t] = cat(lds_tr(b + ((e >> 1) * 40 + (e & 1) * 8) * 128), lds_tr(b + ((e >> 1) * 40 + (e & 1) * 8 + 4) * 128));
        else wf[s & 1][t] = cat(lds_tr(b + 16 * 128), lds_tr(b + 56 * 128));
    };
    rd(0, 0); rd(0, 1);
    wait_flags(P.ws + WS_FLAG2 + (r * 4 + wv) * 8 * 128, 8, lane, epoch);
    STAMP(11);
    auto ld = [&](int s) {
        const int p = s / 5, e = s % 5;
        of[s] = __builtin_amdgcn_raw_buffer_load_b128(wsr, lane * 16 + (e & 3) * 1024, WS_O + ((r * 4 + wv) * 8 + ((p + rot) & 7)) * 5120 + (e >> 2) * 4096, 16);
    };
#pragma unroll
    for (int s = 0; s < O_AHEAD; ++s) ld(s);
    SBAR();
    f32x16 acc[2] = {};
#pragma unroll
    for (int s = 0; s < 40; ++s)
#pragma unroll
        for (int t = 0; t < 2; ++t) {
            if (s < 39) rd(s + 1, t);
            acc[t] = mfma_w(wf[s & 1][t], of[s], acc[t]);
            if (t == 0 && s + O_AHEAD < 40) ld(s + O_AHEAD);
            SBAR();
        }
    STAMP(12);
    lds_ptr st = L + A_OST + wv * 4096;
    const int tok = lane & 31;
#pragma unroll
    for (int c = 0; c < 4; ++c) {
        const f32x16& a = acc[c >> 1];
        const int e = (c & 1) * 8;
        lds_w128(st + tok * 128 + (((2 * c + hi) ^ (tok & 7)) * 16),
                 u32x4{pk(a[e], a[e + 1]), pk(a[e + 2], a[e + 3]), pk(a[e + 4], a[e + 5]), pk(a[e + 6], a[e + 7])});
    }
    FENCE();
    char* ob = P.out + (size_t)(r * 128 + wv * 32 + (lane >> 3)) * 2560 + ct * 128 + (lane & 7) * 16;
#pragma unroll
    for (int j = 0; j < 4; ++j) *(u32x4*)(ob + j * 8 * 2560) = lds_r128(st + j * 1024 + (lane >> 3) * 128 + (((lane & 7) ^ (lane >> 3)) * 16));
    STAMP(13);
}

constexpr int A_WV = 10368, A_X = 20608, A_SLOT = 36992;
constexpr int A_PHI = 0, A_V = 40960;
constexpr int A_WPHI = 157696, A_ZERO = A_WPHI + 3200, A_EPOCH = A_ZERO + 2304;

__device__ __forceinline__ void load_wphi(lds_ptr L, int lane, int t, u32x4 (&wp)[11], lds_ptr pb0, lds_ptr pb1, lds_ptr pb2) {
    const u32x2 z2 = {0u, 0u};
    if (t < 2) {
        wp[3 * t] = cat(lds_tr(pb0 + t * 1600), lds_tr(pb0 + t * 1600 + 160));
        wp[3 * t + 1] = cat(lds_tr(pb0 + t * 1600 + 320), lds_tr(pb0 + t * 1600 + 480));
        wp[3 * t + 2] = t ? cat(z2, lds_tr(pb0 + 1600 + 640)) : cat(lds_tr(pb0 + 640), z2);
    } else if (t < 4) {
        lds_ptr b = t == 2 ? pb1 : pb2;
        wp[2 + 2 * t] = cat(lds_tr(b), lds_tr(b + 160));
        wp[3 + 2 * t] = cat(lds_tr(b + 320), lds_tr(b + 480));
    } else wp[10] = cat(lds_tr(pb1 + 640), lds_tr(pb2 + 640));
}
#define WPHI_BASES() \
    const bool act = hi == (tp & 1); const int gam = 2 * g1 + (tp >> 1); \
    lds_ptr pb0 = opaque(L + (act ? A_WPHI + hi * 800 + tq * 40 + gam * 8 : A_ZERO)); \
    lds_ptr pb1 = opaque(L + (act && gam == 0 ? A_WPHI + hi * 800 + tq * 40 + 32 : A_ZERO)); \
    lds_ptr pb2 = opaque(L + (act && gam == 1 ? A_WPHI + (2 + hi) * 800 + tq * 40 + 32 : A_ZERO))

__device__ __forceinline__ u64 role_k(const Params& P, lds_ptr L, int r, int g, int ct, int rot, int wv STAMP_ARG) {
    const int tid = threadIdx.x, lane = tid & 63;
    const unsigned lds0 = (unsigned)(uintptr_t)L;
    const int n32 = lane & 31, hi = lane >> 5, g1 = (lane >> 4) & 1, tq = (lane & 15) >> 2, tp = lane & 3;
    {
        const char* xsb = P.x + (r * 128 + wv * 32) * 1280;
        set_m0(lds0 + A_X + wv * 4096);
#pragma unroll
        for (int j = 0; j < 4; ++j) dma_off(j * 1024, xsb + j * (8 * 1280 - 1024), (lane >> 3) * 1280 + (((lane & 7) ^ (4 * (j & 1) + (lane >> 4))) * 16));
    }
    if (tid < 200) dma16(P.wphi + g * 3200 + wv * 1024, lane * 16, lds0 + A_WPHI + wv * 1024);
    u64 epoch = ld_agent(P.ws + WS_EPOCH_A);
    __builtin_amdgcn_s_setprio(2);
    if (tid < 144) lds_w128(L + A_ZERO + tid * 16, u32x4{0, 0, 0, 0});

    lds_ptr xa[2][4], wa[2];
#pragma unroll
    for (int h = 0; h < 2; ++h) {
#pragma unroll
        for (int ks = 0; ks < 4; ++ks) xa[h][ks] = opaque(L + h * 2 * A_SLOT + (wv * 32 + n32) * 128 + (((2 * ks + hi) ^ ((n32 >> 1) & 7)) * 16));
        wa[h] = opaque(L + h * 2 * A_SLOT + g1 * A_WV + (8 * hi + tq) * 160 + tp * 40);
    }
    f32x16 acc[5] = {};
    u32x4 xf[2], wf[2][5], wp[11];
    auto rd_main = [&](int s, int t) {
        const int kb = s >> 2, ks = s & 3, h = (kb >> 1) & 1, o = (kb & 1) * A_SLOT;
        if (t == 0) xf[s & 1] = lds_r128(xa[h][ks] + o + A_X);
        wf[s & 1][t] = cat(lds_tr(wa[h] + o + ks * 2560 + t * 8), lds_tr(wa[h] + o + ks * 2560 + 640 + t * 8));
    };
    WPHI_BASES();
    STAMP(1);
    wait_vm<2>();
    bar();
    STAMP(2);
#pragma unroll
    for (int t = 0; t < 5; ++t) rd_main(0, t);
    SBAR();
    static_for<0, 200>([&](auto G) {
        constexpr int gap = G.value, s = gap / 5, t = gap % 5, kb = s >> 2;
        if constexpr ((s & 3) == 3 && t == 0) {
            if constexpr (kb == 2) wait_vm<0>();
            if constexpr (kb < 7 || kb == 8) bar_raw(); else bar();
        }
        if constexpr (s < 39) rd_main(s + 1, t);
        else load_wphi(L, lane, t, wp, pb0, pb1, pb2);
        acc[t] = mfma_w(wf[s & 1][t], xf[s & 1], acc[t]);
        SBAR();
    });
    STAMP(3);
    epoch += 1;
    {
        unsigned X[2][10], F[20];
        float p[2][20];
#pragma unroll
        for (int u = 0; u < 2; ++u)
#pragma unroll
            for (int t = 0; t < 5; ++t) { X[u][2 * t] = pk(acc[t][4 * u], acc[t][4 * u + 1]); X[u][2 * t + 1] = pk(acc[t][4 * u + 2], acc[t][4 * u + 3]); }
        project(X, wp, p);
        const int token = wv * 32 + n32;
        lds_ptr vp = L + A_V + hi * 6144 + token * 48;
#pragma unroll
        for (int u = 0; u < 2; ++u) {
            unsigned V[10];
#pragma unroll
            for (int t = 0; t < 5; ++t) { V[2 * t] = pk(acc[t][8 + 4 * u], acc[t][9 + 4 * u]); V[2 * t + 1] = pk(acc[t][10 + 4 * u], acc[t][11 + 4 * u]); }
            lds_w128(vp + u * 12288, u32x4{V[0], V[1], V[2], V[3]});
            lds_w128(vp + u * 12288 + 16, u32x4{V[4], V[5], V[6], V[7]});
            lds_w128(vp + u * 12288 + 32, u32x4{V[8], V[9], 0x00003f80u, 0u});
        }
        if (tid == 0) lds_w64(L + A_EPOCH, u32x2{(unsigned)epoch, (unsigned)(epoch >> 32)});
        SBAR();
        STAMP(17);
        lds_ptr pp = L + A_PHI + hi * 10240 + token * 80;
#pragma unroll
        for (int u = 0; u < 2; ++u) {
            dual_softmax(p[u], F);
#pragma unroll
            for (int c = 0; c < 5; ++c) lds_w128(pp + u * 20480 + c * 16, u32x4{F[4 * c], F[4 * c + 1], F[4 * c + 2], F[4 * c + 3]});
        }
    }
    const __amdgpu_buffer_rsrc_t wsr = __builtin_amdgcn_make_buffer_rsrc((void*)P.ws, 0, WS_USED, 0x00020000);
    const int set = (g * 4 + wv) * 16;
    {
        const int tok = lane & 15, gq = lane >> 4;
        const int trow = 16 * hi + 8 * g1 + 2 * tq;
        lds_ptr pa = opaque(L + A_PHI + wv * 10240 + trow * 80 + tp * 8);
        lds_ptr pb = opaque(L + A_V + wv * 6144 + trow * 48 + tp * 8);
        const int base = WS_PART + (set + r) * PART_STRIDE;
        int o4a = base + lane * 16;
        int o2a = hi == 0 ? base + 1024 + lane * 8 : WS_USED;
        int o4b = tok < 5 ? base + 1280 + (gq * 5 + tok) * 16 : WS_USED;
        int o2b = (tok < 5 && hi == 0) ? base + 1600 + (gq * 5 + tok) * 8 : WS_USED;
        asm volatile("" : "+v"(o4a), "+v"(o2a), "+v"(o4b), "+v"(o2b));
        STAMP(18);
        bar();
        STAMP(4);
        u32x4 vb[4][2], pf[4][3];
        auto rd = [&](int ks) {
#pragma unroll
            for (int d = 0; d < 2; ++d) vb[ks][d] = cat(lds_tr(pb + ks * 1536 + d * 32), lds_tr(pb + ks * 1536 + 48 + d * 32));
#pragma unroll
            for (int f = 0; f < 3; ++f) pf[ks][f] = cat(lds_tr(pa + ks * 2560 + f * 32), lds_tr(pa + ks * 2560 + 80 + f * 32));
        };
        rd(0);
        SBAR();
        f32x4 kv[2][3] = {};
#pragma unroll
        for (int ks = 0; ks < 4; ++ks) {
            if (ks < 3) rd(ks + 1);
            SBAR();
#pragma unroll
            for (int f = 0; f < 3; ++f)
#pragma unroll
                for (int d = 0; d < 2; ++d) kv[d][f] = mfma32(pf[ks][f], vb[ks][d], kv[d][f]);
            SBAR();
        }
#pragma unroll
        for (int d = 0; d < 2; ++d) {
            const u32x4 s4 = {pk(kv[d][0][0], kv[d][0][1]), pk(kv[d][0][2], kv[d][0][3]), pk(kv[d][1][0], kv[d][1][1]), pk(kv[d][1][2], kv[d][1][3])};
            const u32x2 s2 = {pk(kv[d][2][0], kv[d][2][1]), pk(kv[d][2][2], kv[d][2][3])};
            __builtin_amdgcn_raw_buffer_store_b128(s4, wsr, d ? o4b : o4a, 0, 16);
            __builtin_amdgcn_raw_buffer_store_b64(s2, wsr, d ? o2b : o2a, 0, 16);
        }
    }
    STAMP(5);
    const unsigned wov = wo_voff(lane);
    stage_wo(P, lds0, ct, rot, wv, wov);
    wait_vm<10>();
    if (lane == 0) st_agent(P.ws + WS_FLAG1 + (set + r) * 128, epoch);
    STAMP(6);
    lds_ptr kw[2][2];
    {
        const int a0 = (lane & 15) * 80 + (lane >> 4) * 8;
        int b0, b1;
        if (lane < 16) { b0 = ((2 * lane) & 15) * 80 + 64 + (lane >> 3) * 8; b1 = b0 + 80; }
        else if (lane < 36) { const int e = lane - 16, gg = e / 5; b0 = (16 + e - gg * 5) * 80 + gg * 8; b1 = b0 + 32; }
        else if (lane < 41) { const int e = 2 * (lane - 36), g0 = e / 5, g2 = (e + 1) / 5; b0 = (16 + e - g0 * 5) * 80 + 64 + g0 * 8; b1 = (17 + e - g2 * 5) * 80 + 64 + g2 * 8; }
        else { b0 = 2048 + lane * 16; b1 = b0 + 8; }
        lds_ptr kb = L + A_V + wv * 6144;
        kw[0][0] = opaque(kb + a0); kw[0][1] = opaque(kb + a0 + 32); kw[1][0] = opaque(kb + b0); kw[1][1] = opaque(kb + b1);
    }
    u32x4 sel[2];
    {
        const int sj = (n32 & 3) + 4 * ((n32 >> 3) & 1);
        const unsigned one = (((n32 >> 2) & 1) == hi) ? ((sj & 1) ? 0x3f800000u : 0x00003f80u) : 0u;
#pragma unroll
        for (int m = 0; m < 2; ++m)
#pragma unroll
            for (int e = 0; e < 4; ++e) sel[m][e] = (g1 == m && (sj >> 1) == e) ? one : 0u;
        asm volatile("" : "+v"(sel[0]), "+v"(sel[1]));
    }
    wait_vm<0>();
    STAMP(7);
    wait_flags(P.ws + WS_FLAG1 + set * 128, 16, lane, epoch);
    STAMP(8);
    {
        u32x4 pr[32];
        const int uo0 = lane * 16, uo1 = lane < 41 ? 1024 + lane * 16 : WS_USED;
        auto ld = [&](int e) {
            pr[e] = __builtin_amdgcn_raw_buffer_load_b128(wsr, (e & 1) ? uo1 : uo0, WS_PART + (set + (((e >> 1) + 4 * (r & 3)) & 15)) * PART_STRIDE, 16);
        };
#pragma unroll
        for (int e = 0; e < P_AHEAD; ++e) ld(e);
        SBAR();
        f32x16 s = {};
#pragma unroll
        for (int e = 0; e < 32; ++e) {
            s = mfma_w(sel[e & 1], pr[e], s);
            if (e + P_AHEAD < 32) ld(e + P_AHEAD);
            SBAR();
        }
#pragma unroll
        for (int m = 0; m < 2; ++m) {
            lds_w64(kw[m][0], u32x2{pk(s[8 * m], s[8 * m + 1]), pk(s[8 * m + 2], s[8 * m + 3])});
            lds_w64(kw[m][1], u32x2{pk(s[8 * m + 4], s[8 * m + 5]), pk(s[8 * m + 6], s[8 * m + 7])});
        }
    }
    STAMP(19);
    bar();
    __builtin_amdgcn_s_setprio(0);
    STAMP(9);
    stage_wo(P, lds0, ct, rot, 4 + wv, wov);
    if (r == 0 && g == 0 && wv == 0) wait_flags(P.ws + WS_FLAGH, 32, lane, epoch);
    wait_vm<0>();
    bar_raw();
    return epoch;
}

__device__ __forceinline__ void role_q(const Params& P, lds_ptr L, int r, int g, int wv STAMP_ARG) {
    const int lane = threadIdx.x & 63;
    const unsigned lds0 = (unsigned)(uintptr_t)L;
    const int n32 = lane & 31, hi = lane >> 5, g1 = (lane >> 4) & 1, tq = (lane & 15) >> 2, tp = lane & 3;
    unsigned xo[2], wo5[5];
#pragma unroll
    for (int j = 0; j < 5; ++j) wo5[j] = (((j * 64 + lane) * 6554) >> 16) * (3840 - 160) + lane * 16 + 1024;
    const char* xsb = P.x + (r * 128 + wv * 32) * 1280;
    const char* wsb = P.wqkv + 1280 * (1 + (wv & 1)) + g * 160 + (wv >> 1) * (32 * 3840);
    const char* wqb = P.wqkv + g * 160 + wv * (32 * 3840);
    auto stage_w = [&](const char* s, unsigned d) {
        set_m0(d + 1024);
#pragma unroll
        for (int j = 0; j < 5; ++j) dma_off((j - 1) * 1024, s, wo5[j]);
    };
    auto stage = [&](int n) {
        stage_w(wsb + n * (64 * 3840), lds0 + (wv & 1) * A_WV + (wv >> 1) * 5120 + (n & 3) * A_SLOT);
        if (n > 0) {
            set_m0(lds0 + A_X + wv * 4096 + (n & 3) * A_SLOT);
#pragma unroll
            for (int j = 0; j < 4; ++j) dma_off(j * 1024, xsb + n * 128 + j * (8 * 1280 - 1024), xo[j & 1]);
        }
    };
    stage(0);
#pragma unroll
    for (int j = 0; j < 2; ++j) xo[j] = (lane >> 3) * 1280 + (((lane & 7) ^ (4 * j + (lane >> 4))) * 16);
    stage(1);
    u32x4 xk[40];
    {
        lds_ptr xa[4];
#pragma unroll
        for (int ks = 0; ks < 4; ++ks) xa[ks] = opaque(L + A_X + (wv * 32 + n32) * 128 + (((2 * ks + hi) ^ ((n32 >> 1) & 7)) * 16));
        STAMP(1);
        wait_vm<9>();
        bar();
        STAMP(2);
#pragma unroll
        for (int n = 0; n < 10; ++n) {
#pragma unroll
            for (int ks = 0; ks < 4; ++ks) xk[n * 4 + ks] = lds_r128(xa[ks] + (n & 3) * A_SLOT);
            if (n < 8) { stage(n + 2); wait_vm<9>(); }
            else {
                stage_w(wqb + (n - 8) * 4 * (32 * 3840), lds0 + wq_pos((n - 8) * 4) + wv * 5120);
                wait_vm<5>();
            }
            if (n == 2) STAMP(21);
            if (n == 5) STAMP(22);
            if (n == 8) STAMP(23);
            bar();
        }
    }
    STAMP(3);
#pragma unroll
    for (int h = 2; h < 4; ++h) stage_w(wqb + h * 4 * (32 * 3840), lds0 + wq_pos(h * 4) + wv * 5120);
    if (wv < 2) stage_w(wqb + 16 * (32 * 3840), lds0 + wq_pos(16) + wv * 5120);
    f32x16 acc[3] = {};
    u32x4 wp[11];
    {
        u32x4 wf[2][3];
        lds_ptr qa[2];
#pragma unroll
        for (int h = 0; h < 2; ++h) qa[h] = opaque(L + A_WQ + h * 46080 + (8 * hi + tq) * 160 + tp * 40 + g1 * 8);
        auto rd_q = [&](int s, int t) {
            const int o = wq_pos(s >> 1) - A_WQ, h = o >= 46080;
            lds_ptr b = qa[h] + o - h * 46080 + (s & 1) * 2560 + t * 16;
            wf[s & 1][t] = cat(lds_tr(b), lds_tr(b + 640));
        };
#pragma unroll
        for (int t = 0; t < 3; ++t) rd_q(0, t);
        SBAR();
        static_for<0, 40>([&](auto S) {
            constexpr int s = S.value;
            if constexpr (s == Q_S1 - 1) {
                STAMP(18);
                wait_vm<0>();
                bar();
                STAMP(4);
            }
            if constexpr (s >= Q_S1 && s < Q_S1 + 10) {
                constexpr int e = s - Q_S1;
                if constexpr (e % 5 == 0) set_m0(lds0 + wq_pos(18 + e / 5) + 1024);
                dma_off((e % 5 - 1) * 1024, P.wqkv + g * 160 + (18 + e / 5) * (32 * 3840), wo5[e % 5]);
            }
            if constexpr (s == 35) wait_vm<0>();
#pragma unroll
            for (int t = 0; t < 3; ++t) {
                if constexpr (s < 39) rd_q(s + 1, t);
                acc[t] = mfma_w(wf[s & 1][t], xk[s], acc[t]);
                SBAR();
            }
            if constexpr (s == 36) {
                WPHI_BASES();
#pragma unroll
                for (int t = 0; t < 5; ++t) load_wphi(L, lane, t, wp, pb0, pb1, pb2);
                SBAR();
            }
        });
    }
    STAMP(7);
    unsigned F[2][20];
    {
        unsigned X[2][10];
        float p[2][20];
#pragma unroll
        for (int u = 0; u < 2; ++u)
#pragma unroll
            for (int c = 0; c < 5; ++c) {
                const int e = 4 * (2 * (c & 1) + u);
                X[u][2 * c] = pk(acc[c >> 1][e], acc[c >> 1][e + 1]); X[u][2 * c + 1] = pk(acc[c >> 1][e + 2], acc[c >> 1][e + 3]);
            }
        project(X, wp, p);
        SBAR();
        dual_softmax(p[0], F[0]);
        dual_softmax(p[1], F[1]);
    }
    const u32x2 ev = lds_r64(L + A_EPOCH);
    const __amdgpu_buffer_rsrc_t wsr = __builtin_amdgcn_make_buffer_rsrc((void*)P.ws, 0, WS_USED, 0x00020000);
    const bool on = hi == ((n32 >> 2) & 1);
    const int gm = n32 >> 3, ii = n32 & 3;
    lds_ptr ka[2], kb[2];
#pragma unroll
    for (int u = 0; u < 2; ++u) {
        ka[u] = opaque(L + (on ? A_V + (2 * u + hi) * 6144 + (4 * gm + ii) * 80 : A_ZERO));
        kb[u] = opaque(L + (on && (gm >> 1) == u ? A_V + (2 * u + hi) * 6144 + (16 + 4 * (gm & 1) + ii) * 80 : A_ZERO));
    }
    int ob = WS_O + ((r * 4 + wv) * 8 + g) * 5120 + lane * 16;
    asm volatile("" : "+v"(ob));
    STAMP(14);
    bar();
    STAMP(9);
    u32x4 kf[20];
    auto rd = [&](int e) { kf[e] = lds_r128((e < 10 ? kb[e / 5] : ka[(e - 10) / 5]) + (e % 5) * 16); };
#pragma unroll
    for (int e = 0; e < 6; ++e) rd(e);
    SBAR();
    f32x16 n[3] = {};
    float rd0, rd1;
#pragma unroll
    for (int e = 0; e < 20; ++e) {
        const int u = (e / 5) & 1, f = e % 5, d = e < 10 ? 2 : u;
        if (e + 6 < 20) rd(e + 6);
        n[d] = mfma_w(kf[e], u32x4{F[u][4 * f], F[u][4 * f + 1], F[u][4 * f + 2], F[u][4 * f + 3]}, n[d]);
        if (e == 12) { rd0 = __builtin_amdgcn_rcpf(n[2][4] + 1e-6f); rd1 = __builtin_amdgcn_rcpf(n[2][12] + 1e-6f); }
        if (e == 14)
            __builtin_amdgcn_raw_buffer_store_b128(u32x4{pk(n[2][0] * rd0, n[2][1] * rd0), pk(n[2][2] * rd0, n[2][3] * rd0),
                                                         pk(n[2][8] * rd1, n[2][9] * rd1), pk(n[2][10] * rd1, n[2][11] * rd1)}, wsr, ob + 4096, 0, 16);
        SBAR();
    }
    STAMP(16);
#pragma unroll
    for (int c = 0; c < 4; ++c) {
        const f32x16& a = n[c >> 1];
        const int e = (c & 1) * 8;
        const float rr = (c >> 1) ? rd1 : rd0;
        __builtin_amdgcn_raw_buffer_store_b128(u32x4{pk(a[e] * rr, a[e + 1] * rr), pk(a[e + 2] * rr, a[e + 3] * rr),
                                                     pk(a[e + 4] * rr, a[e + 5] * rr), pk(a[e + 6] * rr, a[e + 7] * rr)}, wsr, ob + c * 1024, 0, 16);
    }
    STAMP(15);
    wait_vm<0>();
    if (lane == 0) st_agent(P.ws + WS_FLAG2 + ((r * 4 + wv) * 8 + g) * 128, ((u64)ev.y << 32) | ev.x);
    STAMP(10);
    wait_vm<0>();
    bar_raw();
}

#define PF_B 8
__device__ __forceinline__ void touch_kv(const Params& P, unsigned dst, int xcd, int q, int lane) {
    const int kb = (q * 3641) >> 16, s = q - kb * 18;
    const bool isx = s < 8;
    const int off = isx ? ((xcd >> 1) * 512 + s * 64) * 1280 + kb * 128
                        : kb * (64 * 3840) + 1280 + (xcd & 1) * 640 + (s - 8) * 128 + (s >= 13 ? 640 : 0);
    touch((isx ? P.x : P.wqkv) + off, lane * (isx ? 1280 : 3840), dst);
}

__device__ __forceinline__ u64 role_h(const Params& P, lds_ptr L, int xcd, int n, int ct, int wv STAMP_ARG) {
    const int lane = threadIdx.x & 63;
    const unsigned lds0 = (unsigned)(uintptr_t)L, dst = lds0 + LDS_BYTES - 256;
    const int id = wv * 6 + n;
    for (int q = PF_B * 10; q < 180; q += 48) {
        if (q + id < 180) touch_kv(P, dst, xcd, q + id, lane);
        bar_raw();
    }
    STAMP(1);
#pragma unroll
    for (int m = 0; m < 2; ++m) {
        const int rs = m ? 1280 : 3840;
        for (int e = id; e < 50; e += 48) {
            const int kb = (e * 13108) >> 16;
            touch((m ? P.wo : P.wqkv) + kb * 64 * rs + (xcd & 1) * 640 + (e - kb * 5) * 128, lane * rs, dst);
        }
        bar_raw();
    }
    for (int e = id; e < 64; e += 48) touch(P.q + (e >> 1) * (64 * 1280) + ((xcd * 160) & ~127) + (e & 1) * 128, lane * 1280, dst);
    STAMP(3);
    if (n >= 4) return 0;
    const u64 epoch = ld_agent(P.ws + WS_EPOCH_A) + 1;
    stage_wo(P, lds0, ct, 1, wv, wo_voff(lane));
    wait_vm<0>();
    bar();
    if (threadIdx.x == 0) st_agent(P.ws + WS_FLAGH + (xcd * 4 + n) * 128, epoch);
    STAMP(2);
    return epoch;
}

constexpr int B_HS = 8512, B_H1 = 64, B_H0 = 64 + 4096 + 132;
constexpr int B_R = 8 * B_HS, B_RS = 9152, B_R0 = 4576;
constexpr int B_K = B_R, B_V = B_R + 33 * 1032;

__device__ __forceinline__ void role_b(const Params& P, lds_ptr L, int xcd, int n, int wv STAMP_ARG) {
    const int tid = threadIdx.x, lane = tid & 63;
    const int grp = xcd * 10 + n, col = grp >> 3, j8 = grp & 7;
    const unsigned lds0 = (unsigned)(uintptr_t)L;
    if (wv < PF_B) touch_kv(P, lds0 + LDS_BYTES - 256, xcd, wv * 10 + n, lane);
    bar_raw();

    {
        const unsigned so = (lane >> 3) * 1280 + (lane & 7) * 16;
        const size_t tb = (size_t)(j8 * 256 + wv * 32) * 1280 + col * 128;
        if (wv == 0) {
            if (j8 > 0) dma16(P.k + tb - 8 * 1280, so, lds0 + B_K);
            else { lds_w64(L + B_K + lane * 16, u32x2{0, 0}); lds_w64(L + B_K + lane * 16 + 8, u32x2{0, 0}); }
        }
        set_m0(lds0 + B_K + (wv * 4 + 1) * 1032);
#pragma unroll
        for (int m = 0; m < 4; ++m) dma_off(m * 1032, P.k + tb + m * (10240 - 1032), so);
        set_m0(lds0 + B_V + wv * 4 * 1032);
#pragma unroll
        for (int m = 0; m < 4; ++m) dma_off(m * 1032, P.v + tb + m * (10240 - 1032), so);
    }
    const int ch = grp * 8 + wv;
    const int tau = lane & 31, cq = wv * 2 + (lane >> 5);
    const u32x4 sw0 = *(const u32x4*)(P.shk + (col * 64 + cq * 4) * 8), sw1 = *(const u32x4*)(P.shk + (col * 64 + cq * 4) * 8 + 16);
    const u64 epoch = ld_agent(P.ws + WS_EPOCH_B + col * 128) + 1;
    const unsigned dval = *(const unsigned short*)(P.dv + ch * 2);
    if (lane < 16) { lds_w32(L + wv * B_HS + lane * 4, 0u); lds_w32(L + wv * B_HS + B_H0 - 64 + lane * 4, 0u); }
    wait_vm<0>();
#pragma unroll
    for (int h = 0; h < 2; ++h) {
        set_m0(lds0 + wv * B_HS + (h ? B_H0 : B_H1));
#pragma unroll
        for (int m = 0; m < 4; ++m) dma_off(m * 1024, P.hk + ch * 4096, lane * 16);
    }
    bar();
    STAMP(1);
    const __amdgpu_buffer_rsrc_t wsr = __builtin_amdgcn_make_buffer_rsrc((void*)P.ws, 0, WS_USED, 0x00020000);
    {
        lds_ptr kp = opaque(L + B_K + tau * 1032 + cq * 8);
        u32x2 kr[11], vr[8];
#pragma unroll
        for (int e = 0; e < 11; ++e) { kr[e] = lds_r64(kp + (e < 3 ? (5 + e) * 128 : 1032 + (e - 3) * 128)); SBAR(); }
#pragma unroll
        for (int e = 0; e < 8; ++e) { vr[e] = lds_r64(kp + (B_V - B_K) + e * 128); SBAR(); }
        bar();
        f32x2 kk[11][2];
#pragma unroll
        for (int e = 0; e < 11; ++e) { kk[e][0] = f32x2{blo(kr[e].x), bhi(kr[e].x)}; kk[e][1] = f32x2{blo(kr[e].y), bhi(kr[e].y)}; }
        f32x2 w[2][4];
        w[0][0] = f32x2{blo(sw0.x), blo(sw0.z)}; w[0][1] = f32x2{bhi(sw0.x), bhi(sw0.z)};
        w[0][2] = f32x2{blo(sw0.y), blo(sw0.w)}; w[0][3] = f32x2{bhi(sw0.y), bhi(sw0.w)};
        w[1][0] = f32x2{blo(sw1.x), blo(sw1.z)}; w[1][1] = f32x2{bhi(sw1.x), bhi(sw1.z)};
        w[1][2] = f32x2{blo(sw1.y), blo(sw1.w)}; w[1][3] = f32x2{bhi(sw1.y), bhi(sw1.w)};
        f32x2 u[8][2];
#pragma unroll
        for (int e = 0; e < 8; ++e) {
            const u32x2 t = vr[e];
#pragma unroll
            for (int h = 0; h < 2; ++h) {
                f32x2 a = kk[e][h] * w[h][3];
                a = __builtin_elementwise_fma(kk[e + 1][h], w[h][2], a);
                a = __builtin_elementwise_fma(kk[e + 2][h], w[h][1], a);
                a = __builtin_elementwise_fma(kk[e + 3][h], w[h][0], a);
                const unsigned tv = h ? t.y : t.x;
                u[e][h] = a * f32x2{blo(tv), bhi(tv)};
            }
        }
        const int uo = (col * 64 + cq * 4) * 4096 + (255 - (j8 * 32 + tau)) * 16;
#pragma unroll
        for (int c = 0; c < 4; ++c) {
            const int h = c >> 1, e = c & 1;
            const u32x4 o = {pk(u[7][h][e], u[6][h][e]), pk(u[5][h][e], u[4][h][e]), pk(u[3][h][e], u[2][h][e]), pk(u[1][h][e], u[0][h][e])};
            __builtin_amdgcn_raw_buffer_store_b128(o, wsr, uo + WS_U + c * 4096, 0, 16);
        }
    }
    STAMP(2);
    lds_ptr R1 = L + B_R + wv * B_RS;
    if (lane < 28) { lds_w128(R1 + 4096 + lane * 16, u32x4{0, 0, 0, 0}); lds_w128(R1 + B_R0 + 4096 + lane * 16, u32x4{0, 0, 0, 0}); }
    wait_vm<0>();
    STAMP(3);
    if (lane == 0) st_agent(P.ws + WS_FLAGB + ((col * 8 + wv) * 8 + j8) * 128, epoch);
    wait_vm<0>();
    wait_flags(P.ws + WS_FLAGB + grp * 8 * 128, 8, lane, epoch);
    if (j8 == 0 && tid == 0) st_agent(P.ws + WS_EPOCH_B + col * 128, epoch);
    STAMP(4);
    {
        u32x4 a[4];
#pragma unroll
        for (int m = 0; m < 4; ++m) a[m] = __builtin_amdgcn_raw_buffer_load_b128(wsr, lane * 16 + m * 1024, WS_U + ch * 4096, 16);
#pragma unroll
        for (int m = 0; m < 4; ++m) lds_w128(R1 + lane * 16 + m * 1024, a[m]);
        FENCE();
#pragma unroll
        for (int m = 0; m < 4; ++m) {
            const unsigned nx = lds_r32(R1 + lane * 16 + m * 1024 + 16);
            const u32x4 s = {__builtin_amdgcn_alignbit(a[m].y, a[m].x, 16), __builtin_amdgcn_alignbit(a[m].z, a[m].y, 16),
                             __builtin_amdgcn_alignbit(a[m].w, a[m].z, 16), __builtin_amdgcn_alignbit(nx, a[m].w, 16)};
            lds_w128(R1 + B_R0 + lane * 16 + m * 1024, s);
        }
    }
    FENCE();
    STAMP(5);

    const int c16 = lane & 15, kq = lane >> 4;
    lds_ptr ub = R1 + ((c16 & 1) ? 0 : B_R0) + 448 - 64 * (c16 >> 1) + 16 * kq;
    lds_ptr hb = L + wv * B_HS + ((c16 & 1) ? B_H1 : B_H0) + 2 * (2 * c16 - 30 + 8 * kq);
    f32x4 y[8];
    u32x4 qv[4];
    {
        const int e = 30 - 2 * c16 - 8 * kq;
        const u32x4 hd = {e == 0 ? dval : 0u, e == 2 ? dval : 0u, e == 4 ? dval : 0u, e == 6 ? dval : 0u};
lds_ptr hb2 = opaque(hb + 8);
        u32x4 U[2][8], H[2][8];
        auto rd_c = [&](int rr, int s) {
            U[rr & 1][s] = lds_r128(ub + 512 * (7 - s) + 64 * rr);
            H[rr & 1][s] = cat(lds_r64(hb + 512 * s + 64 * rr), lds_r64(hb2 + 512 * s + 64 * rr));
        };
#pragma unroll
        for (int s = 0; s < 8; ++s) { rd_c(0, s); SBAR(); }
#pragma unroll
        for (int j = 0; j < 8; ++j) y[j] = mfma32(U[0][j], hd, f32x4{0.f, 0.f, 0.f, 0.f});
        SBAR();
#pragma unroll
        for (int rr = 0; rr < 8; ++rr) {
            if (rr < 4) qv[rr] = *(const u32x4*)(P.q + (size_t)(rr * 512 + tid) * 1280 + grp * 16);
            int cnt = 0;
#pragma unroll
            for (int s = 0; s < 8; ++s)
#pragma unroll
                for (int j = s; j < 8; ++j) {
                    if (rr < 7 && (cnt & 3) == 0 && cnt < 32) rd_c(rr + 1, cnt >> 2);
                    y[j] = mfma32(U[rr & 1][j - s], H[rr & 1][s], y[j]);
                    if ((cnt & 3) == 3) SBAR();
                    ++cnt;
                }
        }
    }
    STAMP(6);
    {
        lds_ptr yp = L + wv * B_HS + 4 * c16 + 128 * kq;
#pragma unroll
        for (int j = 0; j < 8; ++j) {
            lds_w32(yp + 512 * j, pk(y[j][0], y[j][1]));
            lds_w32(yp + 512 * j + 64, pk(y[j][2], y[j][3]));
        }
    }
    bar();
    STAMP(8);
    {
        lds_ptr yr = L + (c16 >> 2) * B_HS + 2 * (wv * 64 + kq * 16 + (lane & 3) * 4);
#pragma unroll
        for (int m = 0; m < 4; ++m) {
            const u32x4 yy = cat(lds_tr(yr + m * 1024), lds_tr(yr + 4 * B_HS + m * 1024));
            u32x4 o;
#pragma unroll
            for (int e = 0; e < 4; ++e) o[e] = pk(blo(yy[e]) * blo(qv[m][e]), bhi(yy[e]) * bhi(qv[m][e]));
            *(u32x4*)(P.out + (size_t)(m * 512 + tid) * 2560 + 1280 + grp * 16) = o;
        }
    }
    STAMP(7);
}

__host__ __device__ constexpr int role_of(int b) { return (b >> 3) < 16 ? 0 : ((b >> 3) < 26 ? 1 : ((b >> 3) < 30 ? 2 : 3)); }

__global__ void __launch_bounds__(512, 2) fused_kernel(Params P) {
    __shared__ __attribute__((aligned(16))) char smem[LDS_BYTES];
    lds_ptr L = (lds_ptr)smem;
    const int xcd = blockIdx.x & 7, i = blockIdx.x >> 3;
    const int wv = __builtin_amdgcn_readfirstlane(threadIdx.x >> 6);
    STAMP(0);
    if (wv == 7) {
        const char* pc; unsigned len;
        asm volatile("s_getpc_b64 %0\nhk_pc:\n\ts_mov_b32 %1, hk_code_end - hk_pc\n\t.subsection 1\nhk_code_end:\n\t.subsection 0" : "=s"(pc), "=s"(len));
        const unsigned o = ((threadIdx.x & 63) * 32 + i) * 128;
        if (o < len && o < 49152) touch(pc, o, (unsigned)(uintptr_t)L + LDS_BYTES - 256);
    }
    const bool is_a = i < 16;
    const int r = (xcd >> 1) * 4 + ((is_a ? i : i - 26) & 3), g = (xcd & 1) * 4 + (i >> 2);
    const int ct = (xcd & 1) * 5 + (is_a ? i >> 2 : 4), rot = is_a ? 2 * (i >> 2) : 1;
    u64 epoch = 0;
    bool out = false;
    if (is_a) {
        if (wv < 4) { epoch = role_k(P, L, r, g, ct, rot, wv STAMP_PASS); out = true; }
        else role_q(P, L, r, g, wv - 4 STAMP_PASS);
    } else if (i < 26) role_b(P, L, xcd, i - 16, wv STAMP_PASS);
    else { epoch = role_h(P, L, xcd, i - 26, ct, wv STAMP_PASS); out = i < 30 && wv < 4; }
    if (out) {
        out_tile(P, L, epoch, r, ct, rot, wv, threadIdx.x & 63 STAMP_PASS);
        if (blockIdx.x == 0 && threadIdx.x == 0) st_agent(P.ws + WS_EPOCH_A, epoch);
    }
}

}

extern "C" void kernel_launch(void* const* d_in, const int* in_sizes, int n_in,
                              void* d_out, int out_size, void* d_ws, size_t ws_size,
                              hipStream_t stream) {
    hk::Params P;
    P.x = (const char*)d_in[0]; P.wqkv = (const char*)d_in[1]; P.wphi = (const char*)d_in[2]; P.wo = (const char*)d_in[3];
    P.q = (const char*)d_in[4]; P.k = (const char*)d_in[5]; P.v = (const char*)d_in[6];
    P.shk = (const char*)d_in[7]; P.hk = (const char*)d_in[8]; P.dv = (const char*)d_in[9];
    P.out = (char*)d_out; P.ws = (char*)d_ws;
    hk::fused_kernel<<<256, 512, 0, stream>>>(P);
}
```

```cpp
#define STAMPS 0
#include <stdio.h>
#include <vector>
#include <algorithm>
#include <hip/hip_runtime.h>
#include <hip/hip_bf16.h>
#include <stdint.h>

namespace hk {

typedef unsigned long long u64;
typedef __attribute__((ext_vector_type(4))) unsigned u32x4;
typedef __attribute__((ext_vector_type(2))) unsigned u32x2;
typedef __attribute__((ext_vector_type(4))) float f32x4;
typedef __attribute__((ext_vector_type(16))) float f32x16;
typedef __attribute__((ext_vector_type(2))) float f32x2;
typedef __attribute__((ext_vector_type(8))) __bf16 bf16x8;
typedef __attribute__((ext_vector_type(2))) __bf16 bf16x2;
typedef __attribute__((ext_vector_type(4))) short s16x4;
typedef __attribute__((address_space(3))) char* lds_ptr;

constexpr int LDS_BYTES = 163840;

constexpr int WS_EPOCH_A = 0;
constexpr int WS_EPOCH_B = 128;
constexpr int WS_FLAGH = 4096;
constexpr int WS_FLAG1 = 65536;
constexpr int WS_FLAG2 = WS_FLAG1 + 65536;
constexpr int WS_FLAGB = WS_FLAG2 + 65536;
constexpr int WS_PART = 512 << 10;
constexpr int PART_STRIDE = 1792;
constexpr int WS_O = 2 << 20;
constexpr int WS_U = 5 << 20;
constexpr int WS_USED = 8 << 20;
constexpr int WS_STAMP = 16 << 20;

struct Params {
    const char* x; const char* wqkv; const char* wphi; const char* wo;
    const char* q; const char* k; const char* v; const char* shk; const char* hk; const char* dv;
    char* out; char* ws;
};

struct Stamps { unsigned c, w; };
#define STAMP_ARG , Stamps& ST
#define STAMP_PASS , ST
#define STAMP(n) do { SBAR(); u64 c_, w_; \
    asm volatile("s_memtime %0\n\ts_memrealtime %1\n\ts_waitcnt lgkmcnt(0)" : "=&s"(c_), "=&s"(w_) :: "memory"); \
    asm volatile("v_writelane_b32 %0, %2, " #n "\n\tv_writelane_b32 %1, %3, " #n : "+v"(ST.c), "+v"(ST.w) : "s"((unsigned)c_), "s"((unsigned)w_)); SBAR(); } while (0)

template <int V> struct ic { static constexpr int value = V; };
template <int B, int E, class Fn> __device__ __forceinline__ void static_for(Fn&& fn) {
    if constexpr (B < E) { fn(ic<B>{}); static_for<B + 1, E>(fn); }
}
#define SBAR() __builtin_amdgcn_sched_barrier(0)
#define FENCE() asm volatile("" ::: "memory")

__device__ __forceinline__ void dma16(const char* sbase, unsigned voff, unsigned lds_dst) {
    asm volatile("s_mov_b32 m0, %2\n\ts_nop 0\n\tglobal_load_lds_dwordx4 %0, %1" :: "v"(voff), "s"(sbase), "s"(lds_dst) : "memory");
}
__device__ __forceinline__ void set_m0(unsigned d) { asm volatile("s_mov_b32 m0, %0\n\ts_nop 0" :: "s"(d) : "memory"); }
__device__ __forceinline__ void dma_off(int off, const char* sbase, unsigned voff) {
    asm volatile("global_load_lds_dwordx4 %0, %1 offset:%2" :: "v"(voff), "s"(sbase), "i"(off) : "memory");
}
__device__ __forceinline__ void wait_vm_n(int n) { asm volatile("s_waitcnt vmcnt(%0)" :: "i"(n) : "memory"); }
__device__ __forceinline__ void bar_raw() { asm volatile("s_barrier" ::: "memory"); }
__device__ __forceinline__ lds_ptr opaque(lds_ptr p) { asm volatile("" : "+v"(p)); return p; }
template <int N> __device__ __forceinline__ void wait_vm() { asm volatile("s_waitcnt vmcnt(%0)" :: "i"(N) : "memory"); }
__device__ __forceinline__ void bar() { asm volatile("s_waitcnt lgkmcnt(0)\n\ts_barrier" ::: "memory"); }
__device__ __forceinline__ void touch(const char* sbase, unsigned voff, unsigned lds_dst) {
    asm volatile("s_mov_b32 m0, %2\n\ts_nop 0\n\tglobal_load_lds_dword %0, %1" :: "v"(voff), "s"(sbase), "s"(lds_dst) : "memory");
}
__device__ __forceinline__ u32x4 lds_r128(lds_ptr p) { return *(__attribute__((address_space(3))) u32x4*)p; }
__device__ __forceinline__ u32x2 lds_r64(lds_ptr p) { return *(__attribute__((address_space(3))) u32x2*)p; }
__device__ __forceinline__ unsigned lds_r32(lds_ptr p) { return *(__attribute__((address_space(3))) unsigned*)p; }
__device__ __forceinline__ void lds_w128(lds_ptr p, u32x4 v) { *(__attribute__((address_space(3))) u32x4*)p = v; }
__device__ __forceinline__ void lds_w64(lds_ptr p, u32x2 v) { *(__attribute__((address_space(3))) u32x2*)p = v; }
__device__ __forceinline__ void lds_w32(lds_ptr p, unsigned v) { *(__attribute__((address_space(3))) unsigned*)p = v; }
__device__ __forceinline__ u32x2 lds_tr(lds_ptr p) {
    return __builtin_bit_cast(u32x2, __builtin_amdgcn_ds_read_tr16_b64_v4i16((__attribute__((address_space(3))) s16x4*)p));
}
__device__ __forceinline__ u32x4 cat(u32x2 a, u32x2 b) { return u32x4{a.x, a.y, b.x, b.y}; }
__device__ __forceinline__ f32x4 mfma32(u32x4 a, u32x4 b, f32x4 c) {
    return __builtin_amdgcn_mfma_f32_16x16x32_bf16(__builtin_bit_cast(bf16x8, a), __builtin_bit_cast(bf16x8, b), c, 0, 0, 0);
}
__device__ __forceinline__ f32x16 mfma_w(u32x4 a, u32x4 b, f32x16 c) {
    return __builtin_amdgcn_mfma_f32_32x32x16_bf16(__builtin_bit_cast(bf16x8, a), __builtin_bit_cast(bf16x8, b), c, 0, 0, 0);
}
__device__ __forceinline__ unsigned pk(float lo, float hi) {
    return __builtin_bit_cast(unsigned, __builtin_convertvector(f32x2{lo, hi}, bf16x2));
}
__device__ __forceinline__ float blo(unsigned x) { return __uint_as_float(x << 16); }
__device__ __forceinline__ float bhi(unsigned x) { return __uint_as_float(x & 0xffff0000u); }

__device__ __forceinline__ u64 ld_agent(const char* p) { return __hip_atomic_load((const u64*)p, __ATOMIC_RELAXED, __HIP_MEMORY_SCOPE_AGENT); }
__device__ __forceinline__ void st_agent(char* p, u64 v) { __hip_atomic_store((u64*)p, v, __ATOMIC_RELAXED, __HIP_MEMORY_SCOPE_AGENT); }

__device__ __forceinline__ void wait_flags(const char* flags, int n, int lane, u64 epoch) {
    const char* p = flags + (lane < n ? lane : 0) * 128;
    for (int spin = 0; spin < (1 << 22); ++spin) {
        u64 f = ld_agent(p);
        if (__all(f == epoch)) break;
        __builtin_amdgcn_s_sleep(1);
    }
    FENCE();
}

#define O_AHEAD 12
#define Q_S1 8
#define P_AHEAD 20
#define MX3(a, b, c) __builtin_fmaxf(__builtin_fmaxf((a), (b)), (c))
#define MN3(a, b, c) __builtin_fminf(__builtin_fminf((a), (b)), (c))

__device__ __forceinline__ void dual_softmax(const float (&p)[20], unsigned (&F)[20]) {
    const float x0 = MX3(p[0], p[1], p[2]), x1 = MX3(p[3], p[4], p[5]), x2 = MX3(p[6], p[7], p[8]), x3 = MX3(p[9], p[10], p[11]);
    const float n0 = MN3(p[0], p[1], p[2]), n1 = MN3(p[3], p[4], p[5]), n2 = MN3(p[6], p[7], p[8]), n3 = MN3(p[9], p[10], p[11]);
    const float x4 = MX3(x0, p[12], p[13]), x5 = MX3(x1, p[14], p[15]), x6 = MX3(x2, p[16], p[17]), x7 = MX3(x3, p[18], p[19]);
    const float n4 = MN3(n0, p[12], p[13]), n5 = MN3(n1, p[14], p[15]), n6 = MN3(n2, p[16], p[17]), n7 = MN3(n3, p[18], p[19]);
    constexpr float L2E = 1.4426950408889634f;
    const float cp = __builtin_fmaxf(MX3(x4, x5, x6), x7) * -L2E, cn = __builtin_fminf(MN3(n4, n5, n6), n7) * L2E;
    f32x2 ep[10], en[10];
#pragma unroll
    for (int i = 0; i < 10; ++i) {
        const f32x2 v = {p[2 * i], p[2 * i + 1]};
        ep[i] = __builtin_elementwise_fma(v, f32x2{L2E, L2E}, f32x2{cp, cp});
        en[i] = __builtin_elementwise_fma(v, f32x2{-L2E, -L2E}, f32x2{cn, cn});
    }
    SBAR();
#pragma unroll
    for (int i = 0; i < 10; ++i) ep[i] = f32x2{__builtin_amdgcn_exp2f(ep[i].x), __builtin_amdgcn_exp2f(ep[i].y)};
    SBAR();
    const f32x2 sp = ((ep[0] + ep[1]) + (ep[2] + ep[3])) + ((ep[4] + ep[5]) + (ep[6] + ep[7])) + (ep[8] + ep[9]);
#pragma unroll
    for (int i = 0; i < 10; ++i) en[i] = f32x2{__builtin_amdgcn_exp2f(en[i].x), __builtin_amdgcn_exp2f(en[i].y)};
    const float rp = __builtin_amdgcn_rcpf(sp.x + sp.y);
    SBAR();
    const f32x2 sn = ((en[0] + en[1]) + (en[2] + en[3])) + ((en[4] + en[5]) + (en[6] + en[7])) + (en[8] + en[9]);
#pragma unroll
    for (int i = 0; i < 10; ++i) ep[i] *= rp;
    const float rn = __builtin_amdgcn_rcpf(sn.x + sn.y);
    SBAR();
#pragma unroll
    for (int i = 0; i < 10; ++i) F[i] = pk(ep[i].x, ep[i].y);
    SBAR();
#pragma unroll
    for (int i = 0; i < 10; ++i) en[i] *= rn;
    SBAR();
#pragma unroll
    for (int i = 0; i < 10; ++i) F[10 + i] = pk(en[i].x, en[i].y);
    SBAR();
}

__device__ __forceinline__ void project(const unsigned (&X)[2][10], const u32x4 (&W)[11], float (&p)[2][20]) {
    const f32x16 z = {};
    const u32x4 xr = {X[0][8], X[0][9], X[1][8], X[1][9]};
    u32x4 xf[2][2];
#pragma unroll
    for (int u = 0; u < 2; ++u)
#pragma unroll
        for (int c = 0; c < 2; ++c) xf[u][c] = u32x4{X[u][4 * c], X[u][4 * c + 1], X[u][4 * c + 2], X[u][4 * c + 3]};
    f32x16 p2 = mfma_w(W[6], xf[0][0], z);
    f32x16 p0 = mfma_w(W[0], xf[0][0], z);
    p2 = mfma_w(W[7], xf[0][1], p2);
    p0 = mfma_w(W[1], xf[0][1], p0);
    p2 = mfma_w(W[8], xf[1][0], p2);
    p2 = mfma_w(W[9], xf[1][1], p2);
    p2 = mfma_w(W[10], xr, p2);
    p0 = mfma_w(W[2], xr, p0);
    f32x16 p1 = mfma_w(W[3], xf[1][0], z);
    p1 = mfma_w(W[4], xf[1][1], p1);
    p1 = mfma_w(W[5], xr, p1);
#pragma unroll
    for (int e = 0; e < 16; ++e) { p[0][e] = p0[e]; p[1][e] = p1[e]; }
#pragma unroll
    for (int e = 0; e < 4; ++e) { p[0][16 + e] = p2[e]; p[1][16 + e] = p2[4 + e]; }
}

constexpr int A_WQ = 65536;
constexpr int wq_pos(int u) { return A_WQ + (u < 16 ? u + 2 : u - 16) * 5120; }
constexpr int wo_pos(int p) { return p < 4 ? p * 10240 : wq_pos(2) + (p - 4) * 10240; }
constexpr int A_OST = wq_pos(10);

__device__ __forceinline__ unsigned wo_voff(int lane) { return (lane >> 3) * 1280 + (((lane & 7) ^ ((lane >> 2) & 4)) * 16) + 4096; }
__device__ __forceinline__ void stage_wo(const Params& P, unsigned lds0, int ct, int rot, int p, unsigned voff) {
    const char* s = P.wo + ct * 128 + ((p + rot) & 7) * (80 * 1280);
    const unsigned d = lds0 + p * 10240 + (p >= 4 ? wo_pos(4) - 40960 : 0);
#pragma unroll
    for (int h = 0; h < 2; ++h) {
        set_m0(d + h * 5120 + 4096);
#pragma unroll
        for (int j = 0; j < 5; ++j) dma_off((j - 4) * 1024, s + h * 51200 + j * (10240 - 1024), voff);
    }
}

__device__ __forceinline__ void out_tile(const Params& P, lds_ptr L, u64 epoch, int r, int ct, int rot, int wv, int lane STAMP_ARG) {
    const __amdgpu_buffer_rsrc_t wsr = __builtin_amdgcn_make_buffer_rsrc((void*)P.ws, 0, WS_USED, 0x00020000);
    const int hi = lane >> 5, g1 = (lane >> 4) & 1, tq = (lane & 15) >> 2, tp = lane & 3;
    lds_ptr wb[2][2];
    const int wo_ = (hi * 20 + tq) * 128 + g1 * 32 + (tp & 1) * 16 + (tp >> 1) * 8;
#pragma unroll
    for (int h = 0; h < 2; ++h)
#pragma unroll
        for (int t = 0; t < 2; ++t) wb[h][t] = opaque(L + wo_pos(4 * h) + wo_ + ((t ^ (tq >> 1)) * 64));
    u32x4 wf[2][2], of[40];
    auto rd = [&](int s, int t) {
        const int p = s / 5, e = s % 5, h = p >> 2;
        lds_ptr b = wb[h][t] + (p & 3) * 10240;
        if (e < 4) wf[s & 1][t] = cat(lds_tr(b + ((e >> 1) * 40 + (e & 1) * 8) * 128), lds_tr(b + ((e >> 1) * 40 + (e & 1) * 8 + 4) * 128));
        else wf[s & 1][t] = cat(lds_tr(b + 16 * 128), lds_tr(b + 56 * 128));
    };
    rd(0, 0); rd(0, 1);
    wait_flags(P.ws + WS_FLAG2 + (r * 4 + wv) * 8 * 128, 8, lane, epoch);
    STAMP(11);
    auto ld = [&](int s) {
        const int p = s / 5, e = s % 5;
        of[s] = __builtin_amdgcn_raw_buffer_load_b128(wsr, lane * 16 + (e & 3) * 1024, WS_O + ((r * 4 + wv) * 8 + ((p + rot) & 7)) * 5120 + (e >> 2) * 4096, 16);
    };
#pragma unroll
    for (int s = 0; s < O_AHEAD; ++s) ld(s);
    SBAR();
    f32x16 acc[2] = {};
#pragma unroll
    for (int s = 0; s < 40; ++s)
#pragma unroll
        for (int t = 0; t < 2; ++t) {
            if (s < 39) rd(s + 1, t);
            acc[t] = mfma_w(wf[s & 1][t], of[s], acc[t]);
            if (t == 0 && s + O_AHEAD < 40) ld(s + O_AHEAD);
            SBAR();
        }
    STAMP(12);
    lds_ptr st = L + A_OST + wv * 4096;
    const int tok = lane & 31;
#pragma unroll
    for (int c = 0; c < 4; ++c) {
        const f32x16& a = acc[c >> 1];
        const int e = (c & 1) * 8;
        lds_w128(st + tok * 128 + (((2 * c + hi) ^ (tok & 7)) * 16),
                 u32x4{pk(a[e], a[e + 1]), pk(a[e + 2], a[e + 3]), pk(a[e + 4], a[e + 5]), pk(a[e + 6], a[e + 7])});
    }
    FENCE();
    char* ob = P.out + (size_t)(r * 128 + wv * 32 + (lane >> 3)) * 2560 + ct * 128 + (lane & 7) * 16;
#pragma unroll
    for (int j = 0; j < 4; ++j) *(u32x4*)(ob + j * 8 * 2560) = lds_r128(st + j * 1024 + (lane >> 3) * 128 + (((lane & 7) ^ (lane >> 3)) * 16));
    STAMP(13);
}

constexpr int A_WV = 10368, A_X = 20608, A_SLOT = 36992;
constexpr int A_PHI = 0, A_V = 40960;
constexpr int A_WPHI = 157696, A_ZERO = A_WPHI + 3200, A_EPOCH = A_ZERO + 2304;

__device__ __forceinline__ void load_wphi(lds_ptr L, int lane, int t, u32x4 (&wp)[11], lds_ptr pb0, lds_ptr pb1, lds_ptr pb2) {
    const u32x2 z2 = {0u, 0u};
    if (t < 2) {
        wp[3 * t] = cat(lds_tr(pb0 + t * 1600), lds_tr(pb0 + t * 1600 + 160));
        wp[3 * t + 1] = cat(lds_tr(pb0 + t * 1600 + 320), lds_tr(pb0 + t * 1600 + 480));
        wp[3 * t + 2] = t ? cat(z2, lds_tr(pb0 + 1600 + 640)) : cat(lds_tr(pb0 + 640), z2);
    } else if (t < 4) {
        lds_ptr b = t == 2 ? pb1 : pb2;
        wp[2 + 2 * t] = cat(lds_tr(b), lds_tr(b + 160));
        wp[3 + 2 * t] = cat(lds_tr(b + 320), lds_tr(b + 480));
    } else wp[10] = cat(lds_tr(pb1 + 640), lds_tr(pb2 + 640));
}
#define WPHI_BASES() \
    const bool act = hi == (tp & 1); const int gam = 2 * g1 + (tp >> 1); \
    lds_ptr pb0 = opaque(L + (act ? A_WPHI + hi * 800 + tq * 40 + gam * 8 : A_ZERO)); \
    lds_ptr pb1 = opaque(L + (act && gam == 0 ? A_WPHI + hi * 800 + tq * 40 + 32 : A_ZERO)); \
    lds_ptr pb2 = opaque(L + (act && gam == 1 ? A_WPHI + (2 + hi) * 800 + tq * 40 + 32 : A_ZERO))

__device__ __forceinline__ u64 role_k(const Params& P, lds_ptr L, int r, int g, int ct, int rot, int wv STAMP_ARG) {
    const int tid = threadIdx.x, lane = tid & 63;
    const unsigned lds0 = (unsigned)(uintptr_t)L;
    const int n32 = lane & 31, hi = lane >> 5, g1 = (lane >> 4) & 1, tq = (lane & 15) >> 2, tp = lane & 3;
    {
        const char* xsb = P.x + (r * 128 + wv * 32) * 1280;
        set_m0(lds0 + A_X + wv * 4096);
#pragma unroll
        for (int j = 0; j < 4; ++j) dma_off(j * 1024, xsb + j * (8 * 1280 - 1024), (lane >> 3) * 1280 + (((lane & 7) ^ (4 * (j & 1) + (lane >> 4))) * 16));
    }
    if (tid < 200) dma16(P.wphi + g * 3200 + wv * 1024, lane * 16, lds0 + A_WPHI + wv * 1024);
    u64 epoch = ld_agent(P.ws + WS_EPOCH_A);
    __builtin_amdgcn_s_setprio(2);
    if (tid < 144) lds_w128(L + A_ZERO + tid * 16, u32x4{0, 0, 0, 0});

    lds_ptr xa[2][4], wa[2];
#pragma unroll
    for (int h = 0; h < 2; ++h) {
#pragma unroll
        for (int ks = 0; ks < 4; ++ks) xa[h][ks] = opaque(L + h * 2 * A_SLOT + (wv * 32 + n32) * 128 + (((2 * ks + hi) ^ ((n32 >> 1) & 7)) * 16));
        wa[h] = opaque(L + h * 2 * A_SLOT + g1 * A_WV + (8 * hi + tq) * 160 + tp * 40);
    }
    f32x16 acc[5] = {};
    u32x4 xf[2], wf[2][5], wp[11];
    auto rd_main = [&](int s, int t) {
        const int kb = s >> 2, ks = s & 3, h = (kb >> 1) & 1, o = (kb & 1) * A_SLOT;
        if (t == 0) xf[s & 1] = lds_r128(xa[h][ks] + o + A_X);
        wf[s & 1][t] = cat(lds_tr(wa[h] + o + ks * 2560 + t * 8), lds_tr(wa[h] + o + ks * 2560 + 640 + t * 8));
    };
    WPHI_BASES();
    STAMP(1);
    wait_vm<2>();
    bar();
    STAMP(2);
#pragma unroll
    for (int t = 0; t < 5; ++t) rd_main(0, t);
    SBAR();
    static_for<0, 200>([&](auto G) {
        constexpr int gap = G.value, s = gap / 5, t = gap % 5, kb = s >> 2;
        if constexpr ((s & 3) == 3 && t == 0) {
            if constexpr (kb == 2) wait_vm<0>();
            if constexpr (kb < 7 || kb == 8) bar_raw(); else bar();
        }
        if constexpr (s < 39) rd_main(s + 1, t);
        else load_wphi(L, lane, t, wp, pb0, pb1, pb2);
        acc[t] = mfma_w(wf[s & 1][t], xf[s & 1], acc[t]);
        SBAR();
    });
    STAMP(3);
    epoch += 1;
    {
        unsigned X[2][10], F[20];
        float p[2][20];
#pragma unroll
        for (int u = 0; u < 2; ++u)
#pragma unroll
            for (int t = 0; t < 5; ++t) { X[u][2 * t] = pk(acc[t][4 * u], acc[t][4 * u + 1]); X[u][2 * t + 1] = pk(acc[t][4 * u + 2], acc[t][4 * u + 3]); }
        project(X, wp, p);
        const int token = wv * 32 + n32;
        lds_ptr vp = L + A_V + hi * 6144 + token * 48;
#pragma unroll
        for (int u = 0; u < 2; ++u) {
            unsigned V[10];
#pragma unroll
            for (int t = 0; t < 5; ++t) { V[2 * t] = pk(acc[t][8 + 4 * u], acc[t][9 + 4 * u]); V[2 * t + 1] = pk(acc[t][10 + 4 * u], acc[t][11 + 4 * u]); }
            lds_w128(vp + u * 12288, u32x4{V[0], V[1], V[2], V[3]});
            lds_w128(vp + u * 12288 + 16, u32x4{V[4], V[5], V[6], V[7]});
            lds_w128(vp + u * 12288 + 32, u32x4{V[8], V[9], 0x00003f80u, 0u});
        }
        if (tid == 0) lds_w64(L + A_EPOCH, u32x2{(unsigned)epoch, (unsigned)(epoch >> 32)});
        SBAR();
        STAMP(17);
        lds_ptr pp = L + A_PHI + hi * 10240 + token * 80;
#pragma unroll
        for (int u = 0; u < 2; ++u) {
            dual_softmax(p[u], F);
#pragma unroll
            for (int c = 0; c < 5; ++c) lds_w128(pp + u * 20480 + c * 16, u32x4{F[4 * c], F[4 * c + 1], F[4 * c + 2], F[4 * c + 3]});
        }
    }
    const __amdgpu_buffer_rsrc_t wsr = __builtin_amdgcn_make_buffer_rsrc((void*)P.ws, 0, WS_USED, 0x00020000);
    const int set = (g * 4 + wv) * 16;
    {
        const int tok = lane & 15, gq = lane >> 4;
        const int trow = 16 * hi + 8 * g1 + 2 * tq;
        lds_ptr pa = opaque(L + A_PHI + wv * 10240 + trow * 80 + tp * 8);
        lds_ptr pb = opaque(L + A_V + wv * 6144 + trow * 48 + tp * 8);
        const int base = WS_PART + (set + r) * PART_STRIDE;
        int o4a = base + lane * 16;
        int o2a = hi == 0 ? base + 1024 + lane * 8 : WS_USED;
        int o4b = tok < 5 ? base + 1280 + (gq * 5 + tok) * 16 : WS_USED;
        int o2b = (tok < 5 && hi == 0) ? base + 1600 + (gq * 5 + tok) * 8 : WS_USED;
        asm volatile("" : "+v"(o4a), "+v"(o2a), "+v"(o4b), "+v"(o2b));
        STAMP(18);
        bar();
        STAMP(4);
        u32x4 vb[4][2], pf[4][3];
        auto rd = [&](int ks) {
#pragma unroll
            for (int d = 0; d < 2; ++d) vb[ks][d] = cat(lds_tr(pb + ks * 1536 + d * 32), lds_tr(pb + ks * 1536 + 48 + d * 32));
#pragma unroll
            for (int f = 0; f < 3; ++f) pf[ks][f] = cat(lds_tr(pa + ks * 2560 + f * 32), lds_tr(pa + ks * 2560 + 80 + f * 32));
        };
        rd(0);
        SBAR();
        f32x4 kv[2][3] = {};
#pragma unroll
        for (int ks = 0; ks < 4; ++ks) {
            if (ks < 3) rd(ks + 1);
            SBAR();
#pragma unroll
            for (int f = 0; f < 3; ++f)
#pragma unroll
                for (int d = 0; d < 2; ++d) kv[d][f] = mfma32(pf[ks][f], vb[ks][d], kv[d][f]);
            SBAR();
        }
#pragma unroll
        for (int d = 0; d < 2; ++d) {
            const u32x4 s4 = {pk(kv[d][0][0], kv[d][0][1]), pk(kv[d][0][2], kv[d][0][3]), pk(kv[d][1][0], kv[d][1][1]), pk(kv[d][1][2], kv[d][1][3])};
            const u32x2 s2 = {pk(kv[d][2][0], kv[d][2][1]), pk(kv[d][2][2], kv[d][2][3])};
            __builtin_amdgcn_raw_buffer_store_b128(s4, wsr, d ? o4b : o4a, 0, 16);
            __builtin_amdgcn_raw_buffer_store_b64(s2, wsr, d ? o2b : o2a, 0, 16);
        }
    }
    STAMP(5);
    const unsigned wov = wo_voff(lane);
    stage_wo(P, lds0, ct, rot, wv, wov);
    wait_vm<10>();
    if (lane == 0) st_agent(P.ws + WS_FLAG1 + (set + r) * 128, epoch);
    STAMP(6);
    lds_ptr kw[2][2];
    {
        const int a0 = (lane & 15) * 80 + (lane >> 4) * 8;
        int b0, b1;
        if (lane < 16) { b0 = ((2 * lane) & 15) * 80 + 64 + (lane >> 3) * 8; b1 = b0 + 80; }
        else if (lane < 36) { const int e = lane - 16, gg = e / 5; b0 = (16 + e - gg * 5) * 80 + gg * 8; b1 = b0 + 32; }
        else if (lane < 41) { const int e = 2 * (lane - 36), g0 = e / 5, g2 = (e + 1) / 5; b0 = (16 + e - g0 * 5) * 80 + 64 + g0 * 8; b1 = (17 + e - g2 * 5) * 80 + 64 + g2 * 8; }
        else { b0 = 2048 + lane * 16; b1 = b0 + 8; }
        lds_ptr kb = L + A_V + wv * 6144;
        kw[0][0] = opaque(kb + a0); kw[0][1] = opaque(kb + a0 + 32); kw[1][0] = opaque(kb + b0); kw[1][1] = opaque(kb + b1);
    }
    u32x4 sel[2];
    {
        const int sj = (n32 & 3) + 4 * ((n32 >> 3) & 1);
        const unsigned one = (((n32 >> 2) & 1) == hi) ? ((sj & 1) ? 0x3f800000u : 0x00003f80u) : 0u;
#pragma unroll
        for (int m = 0; m < 2; ++m)
#pragma unroll
            for (int e = 0; e < 4; ++e) sel[m][e] = (g1 == m && (sj >> 1) == e) ? one : 0u;
        asm volatile("" : "+v"(sel[0]), "+v"(sel[1]));
    }
    wait_vm<0>();
    STAMP(7);
    wait_flags(P.ws + WS_FLAG1 + set * 128, 16, lane, epoch);
    STAMP(8);
    {
        u32x4 pr[32];
        const int uo0 = lane * 16, uo1 = lane < 41 ? 1024 + lane * 16 : WS_USED;
        auto ld = [&](int e) {
            pr[e] = __builtin_amdgcn_raw_buffer_load_b128(wsr, (e & 1) ? uo1 : uo0, WS_PART + (set + (((e >> 1) + 4 * (r & 3)) & 15)) * PART_STRIDE, 16);
        };
#pragma unroll
        for (int e = 0; e < P_AHEAD; ++e) ld(e);
        SBAR();
        f32x16 s = {};
#pragma unroll
        for (int e = 0; e < 32; ++e) {
            s = mfma_w(sel[e & 1], pr[e], s);
            if (e + P_AHEAD < 32) ld(e + P_AHEAD);
            SBAR();
        }
#pragma unroll
        for (int m = 0; m < 2; ++m) {
            lds_w64(kw[m][0], u32x2{pk(s[8 * m], s[8 * m + 1]), pk(s[8 * m + 2], s[8 * m + 3])});
            lds_w64(kw[m][1], u32x2{pk(s[8 * m + 4], s[8 * m + 5]), pk(s[8 * m + 6], s[8 * m + 7])});
        }
    }
    STAMP(19);
    bar();
    __builtin_amdgcn_s_setprio(0);
    STAMP(9);
    stage_wo(P, lds0, ct, rot, 4 + wv, wov);
    if (r == 0 && g == 0 && wv == 0) wait_flags(P.ws + WS_FLAGH, 32, lane, epoch);
    wait_vm<0>();
    bar_raw();
    return epoch;
}

__device__ __forceinline__ void role_q(const Params& P, lds_ptr L, int r, int g, int wv STAMP_ARG) {
    const int lane = threadIdx.x & 63;
    const unsigned lds0 = (unsigned)(uintptr_t)L;
    const int n32 = lane & 31, hi = lane >> 5, g1 = (lane >> 4) & 1, tq = (lane & 15) >> 2, tp = lane & 3;
    unsigned xo[2], wo5[5];
#pragma unroll
    for (int j = 0; j < 5; ++j) wo5[j] = (((j * 64 + lane) * 6554) >> 16) * (3840 - 160) + lane * 16 + 1024;
    const char* xsb = P.x + (r * 128 + wv * 32) * 1280;
    const char* wsb = P.wqkv + 1280 * (1 + (wv & 1)) + g * 160 + (wv >> 1) * (32 * 3840);
    const char* wqb = P.wqkv + g * 160 + wv * (32 * 3840);
    auto stage_w = [&](const char* s, unsigned d) {
        set_m0(d + 1024);
#pragma unroll
        for (int j = 0; j < 5; ++j) dma_off((j - 1) * 1024, s, wo5[j]);
    };
    auto stage = [&](int n) {
        stage_w(wsb + n * (64 * 3840), lds0 + (wv & 1) * A_WV + (wv >> 1) * 5120 + (n & 3) * A_SLOT);
        if (n > 0) {
            set_m0(lds0 + A_X + wv * 4096 + (n & 3) * A_SLOT);
#pragma unroll
            for (int j = 0; j < 4; ++j) dma_off(j * 1024, xsb + n * 128 + j * (8 * 1280 - 1024), xo[j & 1]);
        }
    };
    stage(0);
#pragma unroll
    for (int j = 0; j < 2; ++j) xo[j] = (lane >> 3) * 1280 + (((lane & 7) ^ (4 * j + (lane >> 4))) * 16);
    stage(1);
    u32x4 xk[40];
    {
        lds_ptr xa[4];
#pragma unroll
        for (int ks = 0; ks < 4; ++ks) xa[ks] = opaque(L + A_X + (wv * 32 + n32) * 128 + (((2 * ks + hi) ^ ((n32 >> 1) & 7)) * 16));
        STAMP(1);
        wait_vm<9>();
        bar();
        STAMP(2);
#pragma unroll
        for (int n = 0; n < 10; ++n) {
#pragma unroll
            for (int ks = 0; ks < 4; ++ks) xk[n * 4 + ks] = lds_r128(xa[ks] + (n & 3) * A_SLOT);
            if (n < 8) { stage(n + 2); wait_vm<9>(); }
            else {
                stage_w(wqb + (n - 8) * 4 * (32 * 3840), lds0 + wq_pos((n - 8) * 4) + wv * 5120);
                wait_vm<5>();
            }
            if (n == 2) STAMP(21);
            if (n == 5) STAMP(22);
            if (n == 8) STAMP(23);
            bar();
        }
    }
    STAMP(3);
#pragma unroll
    for (int h = 2; h < 4; ++h) stage_w(wqb + h * 4 * (32 * 3840), lds0 + wq_pos(h * 4) + wv * 5120);
    if (wv < 2) stage_w(wqb + 16 * (32 * 3840), lds0 + wq_pos(16) + wv * 5120);
    f32x16 acc[3] = {};
    u32x4 wp[11];
    {
        u32x4 wf[2][3];
        lds_ptr qa[2];
#pragma unroll
        for (int h = 0; h < 2; ++h) qa[h] = opaque(L + A_WQ + h * 46080 + (8 * hi + tq) * 160 + tp * 40 + g1 * 8);
        auto rd_q = [&](int s, int t) {
            const int o = wq_pos(s >> 1) - A_WQ, h = o >= 46080;
            lds_ptr b = qa[h] + o - h * 46080 + (s & 1) * 2560 + t * 16;
            wf[s & 1][t] = cat(lds_tr(b), lds_tr(b + 640));
        };
#pragma unroll
        for (int t = 0; t < 3; ++t) rd_q(0, t);
        SBAR();
        static_for<0, 40>([&](auto S) {
            constexpr int s = S.value;
            if constexpr (s == Q_S1 - 1) {
                STAMP(18);
                wait_vm<0>();
                bar();
                STAMP(4);
            }
            if constexpr (s >= Q_S1 && s < Q_S1 + 10) {
                constexpr int e = s - Q_S1;
                if constexpr (e % 5 == 0) set_m0(lds0 + wq_pos(18 + e / 5) + 1024);
                dma_off((e % 5 - 1) * 1024, P.wqkv + g * 160 + (18 + e / 5) * (32 * 3840), wo5[e % 5]);
            }
            if constexpr (s == 35) wait_vm<0>();
#pragma unroll
            for (int t = 0; t < 3; ++t) {
                if constexpr (s < 39) rd_q(s + 1, t);
                acc[t] = mfma_w(wf[s & 1][t], xk[s], acc[t]);
                SBAR();
            }
            if constexpr (s == 36) {
                WPHI_BASES();
#pragma unroll
                for (int t = 0; t < 5; ++t) load_wphi(L, lane, t, wp, pb0, pb1, pb2);
                SBAR();
            }
        });
    }
    STAMP(7);
    unsigned F[2][20];
    {
        unsigned X[2][10];
        float p[2][20];
#pragma unroll
        for (int u = 0; u < 2; ++u)
#pragma unroll
            for (int c = 0; c < 5; ++c) {
                const int e = 4 * (2 * (c & 1) + u);
                X[u][2 * c] = pk(acc[c >> 1][e], acc[c >> 1][e + 1]); X[u][2 * c + 1] = pk(acc[c >> 1][e + 2], acc[c >> 1][e + 3]);
            }
        project(X, wp, p);
        SBAR();
        dual_softmax(p[0], F[0]);
        dual_softmax(p[1], F[1]);
    }
    const u32x2 ev = lds_r64(L + A_EPOCH);
    const __amdgpu_buffer_rsrc_t wsr = __builtin_amdgcn_make_buffer_rsrc((void*)P.ws, 0, WS_USED, 0x00020000);
    const bool on = hi == ((n32 >> 2) & 1);
    const int gm = n32 >> 3, ii = n32 & 3;
    lds_ptr ka[2], kb[2];
#pragma unroll
    for (int u = 0; u < 2; ++u) {
        ka[u] = opaque(L + (on ? A_V + (2 * u + hi) * 6144 + (4 * gm + ii) * 80 : A_ZERO));
        kb[u] = opaque(L + (on && (gm >> 1) == u ? A_V + (2 * u + hi) * 6144 + (16 + 4 * (gm & 1) + ii) * 80 : A_ZERO));
    }
    int ob = WS_O + ((r * 4 + wv) * 8 + g) * 5120 + lane * 16;
    asm volatile("" : "+v"(ob));
    STAMP(14);
    bar();
    STAMP(9);
    u32x4 kf[20];
    auto rd = [&](int e) { kf[e] = lds_r128((e < 10 ? kb[e / 5] : ka[(e - 10) / 5]) + (e % 5) * 16); };
#pragma unroll
    for (int e = 0; e < 6; ++e) rd(e);
    SBAR();
    f32x16 n[3] = {};
    float rd0, rd1;
#pragma unroll
    for (int e = 0; e < 20; ++e) {
        const int u = (e / 5) & 1, f = e % 5, d = e < 10 ? 2 : u;
        if (e + 6 < 20) rd(e + 6);
        n[d] = mfma_w(kf[e], u32x4{F[u][4 * f], F[u][4 * f + 1], F[u][4 * f + 2], F[u][4 * f + 3]}, n[d]);
        if (e == 12) { rd0 = __builtin_amdgcn_rcpf(n[2][4] + 1e-6f); rd1 = __builtin_amdgcn_rcpf(n[2][12] + 1e-6f); }
        if (e == 14)
            __builtin_amdgcn_raw_buffer_store_b128(u32x4{pk(n[2][0] * rd0, n[2][1] * rd0), pk(n[2][2] * rd0, n[2][3] * rd0),
                                                         pk(n[2][8] * rd1, n[2][9] * rd1), pk(n[2][10] * rd1, n[2][11] * rd1)}, wsr, ob + 4096, 0, 16);
        SBAR();
    }
    STAMP(16);
#pragma unroll
    for (int c = 0; c < 4; ++c) {
        const f32x16& a = n[c >> 1];
        const int e = (c & 1) * 8;
        const float rr = (c >> 1) ? rd1 : rd0;
        __builtin_amdgcn_raw_buffer_store_b128(u32x4{pk(a[e] * rr, a[e + 1] * rr), pk(a[e + 2] * rr, a[e + 3] * rr),
                                                     pk(a[e + 4] * rr, a[e + 5] * rr), pk(a[e + 6] * rr, a[e + 7] * rr)}, wsr, ob + c * 1024, 0, 16);
    }
    STAMP(15);
    wait_vm<0>();
    if (lane == 0) st_agent(P.ws + WS_FLAG2 + ((r * 4 + wv) * 8 + g) * 128, ((u64)ev.y << 32) | ev.x);
    STAMP(10);
    wait_vm<0>();
    bar_raw();
}

#define PF_B 8
__device__ __forceinline__ void touch_kv(const Params& P, unsigned dst, int xcd, int q, int lane) {
    const int kb = (q * 3641) >> 16, s = q - kb * 18;
    const bool isx = s < 8;
    const int off = isx ? ((xcd >> 1) * 512 + s * 64) * 1280 + kb * 128
                        : kb * (64 * 3840) + 1280 + (xcd & 1) * 640 + (s - 8) * 128 + (s >= 13 ? 640 : 0);
    touch((isx ? P.x : P.wqkv) + off, lane * (isx ? 1280 : 3840), dst);
}

__device__ __forceinline__ u64 role_h(const Params& P, lds_ptr L, int xcd, int n, int ct, int wv STAMP_ARG) {
    const int lane = threadIdx.x & 63;
    const unsigned lds0 = (unsigned)(uintptr_t)L, dst = lds0 + LDS_BYTES - 256;
    const int id = wv * 6 + n;
    for (int q = PF_B * 10; q < 180; q += 48) {
        if (q + id < 180) touch_kv(P, dst, xcd, q + id, lane);
        bar_raw();
    }
    STAMP(1);
#pragma unroll
    for (int m = 0; m < 2; ++m) {
        const int rs = m ? 1280 : 3840;
        for (int e = id; e < 50; e += 48) {
            const int kb = (e * 13108) >> 16;
            touch((m ? P.wo : P.wqkv) + kb * 64 * rs + (xcd & 1) * 640 + (e - kb * 5) * 128, lane * rs, dst);
        }
        bar_raw();
    }
    for (int e = id; e < 64; e += 48) touch(P.q + (e >> 1) * (64 * 1280) + ((xcd * 160) & ~127) + (e & 1) * 128, lane * 1280, dst);
    STAMP(3);
    if (n >= 4) return 0;
    const u64 epoch = ld_agent(P.ws + WS_EPOCH_A) + 1;
    stage_wo(P, lds0, ct, 1, wv, wo_voff(lane));
    wait_vm<0>();
    bar();
    if (threadIdx.x == 0) st_agent(P.ws + WS_FLAGH + (xcd * 4 + n) * 128, epoch);
    STAMP(2);
    return epoch;
}

constexpr int B_HS = 8512, B_H1 = 64, B_H0 = 64 + 4096 + 132;
constexpr int B_R = 8 * B_HS, B_RS = 9152, B_R0 = 4576;
constexpr int B_K = B_R, B_V = B_R + 33 * 1032;

__device__ __forceinline__ void role_b(const Params& P, lds_ptr L, int xcd, int n, int wv STAMP_ARG) {
    const int tid = threadIdx.x, lane = tid & 63;
    const int grp = xcd * 10 + n, col = grp >> 3, j8 = grp & 7;
    const unsigned lds0 = (unsigned)(uintptr_t)L;
    if (wv < PF_B) touch_kv(P, lds0 + LDS_BYTES - 256, xcd, wv * 10 + n, lane);
    bar_raw();

    {
        const unsigned so = (lane >> 3) * 1280 + (lane & 7) * 16;
        const size_t tb = (size_t)(j8 * 256 + wv * 32) * 1280 + col * 128;
        if (wv == 0) {
            if (j8 > 0) dma16(P.k + tb - 8 * 1280, so, lds0 + B_K);
            else { lds_w64(L + B_K + lane * 16, u32x2{0, 0}); lds_w64(L + B_K + lane * 16 + 8, u32x2{0, 0}); }
        }
        set_m0(lds0 + B_K + (wv * 4 + 1) * 1032);
#pragma unroll
        for (int m = 0; m < 4; ++m) dma_off(m * 1032, P.k + tb + m * (10240 - 1032), so);
        set_m0(lds0 + B_V + wv * 4 * 1032);
#pragma unroll
        for (int m = 0; m < 4; ++m) dma_off(m * 1032, P.v + tb + m * (10240 - 1032), so);
    }
    const int ch = grp * 8 + wv;
    const int tau = lane & 31, cq = wv * 2 + (lane >> 5);
    const u32x4 sw0 = *(const u32x4*)(P.shk + (col * 64 + cq * 4) * 8), sw1 = *(const u32x4*)(P.shk + (col * 64 + cq * 4) * 8 + 16);
    const u64 epoch = ld_agent(P.ws + WS_EPOCH_B + col * 128) + 1;
    const unsigned dval = *(const unsigned short*)(P.dv + ch * 2);
    if (lane < 16) { lds_w32(L + wv * B_HS + lane * 4, 0u); lds_w32(L + wv * B_HS + B_H0 - 64 + lane * 4, 0u); }
    wait_vm<0>();
#pragma unroll
    for (int h = 0; h < 2; ++h) {
        set_m0(lds0 + wv * B_HS + (h ? B_H0 : B_H1));
#pragma unroll
        for (int m = 0; m < 4; ++m) dma_off(m * 1024, P.hk + ch * 4096, lane * 16);
    }
    bar();
    STAMP(1);
    const __amdgpu_buffer_rsrc_t wsr = __builtin_amdgcn_make_buffer_rsrc((void*)P.ws, 0, WS_USED, 0x00020000);
    {
        lds_ptr kp = opaque(L + B_K + tau * 1032 + cq * 8);
        u32x2 kr[11], vr[8];
#pragma unroll
        for (int e = 0; e < 11; ++e) { kr[e] = lds_r64(kp + (e < 3 ? (5 + e) * 128 : 1032 + (e - 3) * 128)); SBAR(); }
#pragma unroll
        for (int e = 0; e < 8; ++e) { vr[e] = lds_r64(kp + (B_V - B_K) + e * 128); SBAR(); }
        bar();
        f32x2 kk[11][2];
#pragma unroll
        for (int e = 0; e < 11; ++e) { kk[e][0] = f32x2{blo(kr[e].x), bhi(kr[e].x)}; kk[e][1] = f32x2{blo(kr[e].y), bhi(kr[e].y)}; }
        f32x2 w[2][4];
        w[0][0] = f32x2{blo(sw0.x), blo(sw0.z)}; w[0][1] = f32x2{bhi(sw0.x), bhi(sw0.z)};
        w[0][2] = f32x2{blo(sw0.y), blo(sw0.w)}; w[0][3] = f32x2{bhi(sw0.y), bhi(sw0.w)};
        w[1][0] = f32x2{blo(sw1.x), blo(sw1.z)}; w[1][1] = f32x2{bhi(sw1.x), bhi(sw1.z)};
        w[1][2] = f32x2{blo(sw1.y), blo(sw1.w)}; w[1][3] = f32x2{bhi(sw1.y), bhi(sw1.w)};
        f32x2 u[8][2];
#pragma unroll
        for (int e = 0; e < 8; ++e) {
            const u32x2 t = vr[e];
#pragma unroll
            for (int h = 0; h < 2; ++h) {
                f32x2 a = kk[e][h] * w[h][3];
                a = __builtin_elementwise_fma(kk[e + 1][h], w[h][2], a);
                a = __builtin_elementwise_fma(kk[e + 2][h], w[h][1], a);
                a = __builtin_elementwise_fma(kk[e + 3][h], w[h][0], a);
                const unsigned tv = h ? t.y : t.x;
                u[e][h] = a * f32x2{blo(tv), bhi(tv)};
            }
        }
        const int uo = (col * 64 + cq * 4) * 4096 + (255 - (j8 * 32 + tau)) * 16;
#pragma unroll
        for (int c = 0; c < 4; ++c) {
            const int h = c >> 1, e = c & 1;
            const u32x4 o = {pk(u[7][h][e], u[6][h][e]), pk(u[5][h][e], u[4][h][e]), pk(u[3][h][e], u[2][h][e]), pk(u[1][h][e], u[0][h][e])};
            __builtin_amdgcn_raw_buffer_store_b128(o, wsr, uo + WS_U + c * 4096, 0, 16);
        }
    }
    STAMP(2);
    lds_ptr R1 = L + B_R + wv * B_RS;
    if (lane < 28) { lds_w128(R1 + 4096 + lane * 16, u32x4{0, 0, 0, 0}); lds_w128(R1 + B_R0 + 4096 + lane * 16, u32x4{0, 0, 0, 0}); }
    wait_vm<0>();
    STAMP(3);
    if (lane == 0) st_agent(P.ws + WS_FLAGB + ((col * 8 + wv) * 8 + j8) * 128, epoch);
    wait_vm<0>();
    wait_flags(P.ws + WS_FLAGB + grp * 8 * 128, 8, lane, epoch);
    if (j8 == 0 && tid == 0) st_agent(P.ws + WS_EPOCH_B + col * 128, epoch);
    STAMP(4);
    {
        u32x4 a[4];
#pragma unroll
        for (int m = 0; m < 4; ++m) a[m] = __builtin_amdgcn_raw_buffer_load_b128(wsr, lane * 16 + m * 1024, WS_U + ch * 4096, 16);
#pragma unroll
        for (int m = 0; m < 4; ++m) lds_w128(R1 + lane * 16 + m * 1024, a[m]);
        FENCE();
#pragma unroll
        for (int m = 0; m < 4; ++m) {
            const unsigned nx = lds_r32(R1 + lane * 16 + m * 1024 + 16);
            const u32x4 s = {__builtin_amdgcn_alignbit(a[m].y, a[m].x, 16), __builtin_amdgcn_alignbit(a[m].z, a[m].y, 16),
                             __builtin_amdgcn_alignbit(a[m].w, a[m].z, 16), __builtin_amdgcn_alignbit(nx, a[m].w, 16)};
            lds_w128(R1 + B_R0 + lane * 16 + m * 1024, s);
        }
    }
    FENCE();
    STAMP(5);

    const int c16 = lane & 15, kq = lane >> 4;
    lds_ptr ub = R1 + ((c16 & 1) ? 0 : B_R0) + 448 - 64 * (c16 >> 1) + 16 * kq;
    lds_ptr hb = L + wv * B_HS + ((c16 & 1) ? B_H1 : B_H0) + 2 * (2 * c16 - 30 + 8 * kq);
    f32x4 y[8];
    u32x4 qv[4];
    {
        const int e = 30 - 2 * c16 - 8 * kq;
        const u32x4 hd = {e == 0 ? dval : 0u, e == 2 ? dval : 0u, e == 4 ? dval : 0u, e == 6 ? dval : 0u};
lds_ptr hb2 = opaque(hb + 8);
        u32x4 U[2][8], H[2][8];
        auto rd_c = [&](int rr, int s) {
            U[rr & 1][s] = lds_r128(ub + 512 * (7 - s) + 64 * rr);
            H[rr & 1][s] = cat(lds_r64(hb + 512 * s + 64 * rr), lds_r64(hb2 + 512 * s + 64 * rr));
        };
#pragma unroll
        for (int s = 0; s < 8; ++s) { rd_c(0, s); SBAR(); }
#pragma unroll
        for (int j = 0; j < 8; ++j) y[j] = mfma32(U[0][j], hd, f32x4{0.f, 0.f, 0.f, 0.f});
        SBAR();
#pragma unroll
        for (int rr = 0; rr < 8; ++rr) {
            if (rr < 4) qv[rr] = *(const u32x4*)(P.q + (size_t)(rr * 512 + tid) * 1280 + grp * 16);
            int cnt = 0;
#pragma unroll
            for (int s = 0; s < 8; ++s)
#pragma unroll
                for (int j = s; j < 8; ++j) {
                    if (rr < 7 && (cnt & 3) == 0 && cnt < 32) rd_c(rr + 1, cnt >> 2);
                    y[j] = mfma32(U[rr & 1][j - s], H[rr & 1][s], y[j]);
                    if ((cnt & 3) == 3) SBAR();
                    ++cnt;
                }
        }
    }
    STAMP(6);
    {
        lds_ptr yp = L + wv * B_HS + 4 * c16 + 128 * kq;
#pragma unroll
        for (int j = 0; j < 8; ++j) {
            lds_w32(yp + 512 * j, pk(y[j][0], y[j][1]));
            lds_w32(yp + 512 * j + 64, pk(y[j][2], y[j][3]));
        }
    }
    bar();
    STAMP(8);
    {
        lds_ptr yr = L + (c16 >> 2) * B_HS + 2 * (wv * 64 + kq * 16 + (lane & 3) * 4);
#pragma unroll
        for (int m = 0; m < 4; ++m) {
            const u32x4 yy = cat(lds_tr(yr + m * 1024), lds_tr(yr + 4 * B_HS + m * 1024));
            u32x4 o;
#pragma unroll
            for (int e = 0; e < 4; ++e) o[e] = pk(blo(yy[e]) * blo(qv[m][e]), bhi(yy[e]) * bhi(qv[m][e]));
            *(u32x4*)(P.out + (size_t)(m * 512 + tid) * 2560 + 1280 + grp * 16) = o;
        }
    }
    STAMP(7);
}

__host__ __device__ constexpr int role_of(int b) { return (b >> 3) < 16 ? 0 : ((b >> 3) < 26 ? 1 : ((b >> 3) < 30 ? 2 : 3)); }

__global__ void __launch_bounds__(512, 2) fused_kernel(Params P) {
    __shared__ __attribute__((aligned(16))) char smem[LDS_BYTES];
    lds_ptr L = (lds_ptr)smem;
    const int xcd = blockIdx.x & 7, i = blockIdx.x >> 3;
    const int wv = __builtin_amdgcn_readfirstlane(threadIdx.x >> 6);
    Stamps ST = {0u, 0u};
    STAMP(0);
    if (wv == 7) {
        const char* pc; unsigned len;
        asm volatile("s_getpc_b64 %0\nhk_pc:\n\ts_mov_b32 %1, hk_code_end - hk_pc\n\t.subsection 1\nhk_code_end:\n\t.subsection 0" : "=s"(pc), "=s"(len));
        const unsigned o = ((threadIdx.x & 63) * 32 + i) * 128;
        if (o < len && o < 49152) touch(pc, o, (unsigned)(uintptr_t)L + LDS_BYTES - 256);
    }
    asm volatile("" :: "s"(P.x), "s"(P.ws));
    STAMP(30);
    const bool is_a = i < 16;
    const int r = (xcd >> 1) * 4 + ((is_a ? i : i - 26) & 3), g = (xcd & 1) * 4 + (i >> 2);
    const int ct = (xcd & 1) * 5 + (is_a ? i >> 2 : 4), rot = is_a ? 2 * (i >> 2) : 1;
    u64 epoch = 0;
    bool out = false;
    if (is_a) {
        if (wv < 4) { epoch = role_k(P, L, r, g, ct, rot, wv STAMP_PASS); out = true; }
        else role_q(P, L, r, g, wv - 4 STAMP_PASS);
    } else if (i < 26) role_b(P, L, xcd, i - 16, wv STAMP_PASS);
    else { epoch = role_h(P, L, xcd, i - 26, ct, wv STAMP_PASS); out = i < 30 && wv < 4; }
    if (out) {
        out_tile(P, L, epoch, r, ct, rot, wv, threadIdx.x & 63 STAMP_PASS);
        if (blockIdx.x == 0 && threadIdx.x == 0) st_agent(P.ws + WS_EPOCH_A, epoch);
    }
    wait_vm<0>();
    STAMP(31);
    if ((threadIdx.x >> 6) == STAMPS && (threadIdx.x & 63) < 32) {
        ((long long*)(P.ws + WS_STAMP))[blockIdx.x * 32 + (threadIdx.x & 63)] = ST.w;
        ((long long*)(P.ws + WS_STAMP + 65536))[blockIdx.x * 32 + (threadIdx.x & 63)] = ST.c;
    }
}

}

extern "C" void kernel_launch(void* const* d_in, const int* in_sizes, int n_in,
                              void* d_out, int out_size, void* d_ws, size_t ws_size,
                              hipStream_t stream) {
    hk::Params P;
    P.x = (const char*)d_in[0]; P.wqkv = (const char*)d_in[1]; P.wphi = (const char*)d_in[2]; P.wo = (const char*)d_in[3];
    P.q = (const char*)d_in[4]; P.k = (const char*)d_in[5]; P.v = (const char*)d_in[6];
    P.shk = (const char*)d_in[7]; P.hk = (const char*)d_in[8]; P.dv = (const char*)d_in[9];
    P.out = (char*)d_out; P.ws = (char*)d_ws;
    hk::fused_kernel<<<256, 512, 0, stream>>>(P);
    hipStreamCaptureStatus cs = hipStreamCaptureStatusNone;
    (void)hipStreamIsCapturing(stream, &cs);
    if (cs == hipStreamCaptureStatusNone) {
        auto dump = [&](const char* tag) {
            (void)hipStreamSynchronize(stream);
            std::vector<long long> s(256 * 32), c(256 * 32);
            (void)hipMemcpy(s.data(), P.ws + hk::WS_STAMP, 256 * 32 * 8, hipMemcpyDeviceToHost);
            (void)hipMemcpy(c.data(), P.ws + hk::WS_STAMP + 65536, 256 * 32 * 8, hipMemcpyDeviceToHost);
            long long mn = 1LL << 62;
            for (int b = 0; b < 256; ++b) mn = std::min(mn, s[b * 32]);
            for (int role = 0; role < 3; ++role) {
                std::vector<std::pair<double, int>> ord; double mx[32];
                for (int j = 0; j < 32; ++j) {
                    std::vector<long long> v;
                    for (int b = 0; b < 256; ++b) if (hk::role_of(b) == role && s[b * 32 + j] != 0) v.push_back(s[b * 32 + j] - mn);
                    if (v.size() < 8) continue;
                    std::sort(v.begin(), v.end()); ord.push_back({v[v.size() / 2] / 100.0, j}); mx[j] = v.back() / 100.0;
                }
                std::sort(ord.begin(), ord.end());
                fprintf(stderr, "%s %c:", tag, "ABH"[role]);
                for (auto& o : ord) fprintf(stderr, " [%d] %.2f/%.2f", o.second, o.first, mx[o.second]);
                fprintf(stderr, " clk %.2f\n", (double)(c[31] - c[0]) / (s[31] - s[0]) / 10.0);
            }
        };
        fprintf(stderr, "ptrs x %p w %p q %p k %p hk %p out %p ws %p\n", P.x, P.wqkv, P.q, P.k, P.hk, P.out, P.ws);
        dump("first");
        hk::fused_kernel<<<256, 512, 0, stream>>>(P);
        dump("idle");
        for (int j = 0; j < 20; ++j) hk::fused_kernel<<<256, 512, 0, stream>>>(P);
        dump("b2b");
    }

}
```

```cpp
#define STAMPS 0
#include <hip/hip_runtime.h>
#include <hip/hip_bf16.h>
#include <stdint.h>

namespace hk {

typedef unsigned long long u64;
typedef __attribute__((ext_vector_type(4))) unsigned u32x4;
typedef __attribute__((ext_vector_type(2))) unsigned u32x2;
typedef __attribute__((ext_vector_type(4))) float f32x4;
typedef __attribute__((ext_vector_type(16))) float f32x16;
typedef __attribute__((ext_vector_type(2))) float f32x2;
typedef __attribute__((ext_vector_type(8))) __bf16 bf16x8;
typedef __attribute__((ext_vector_type(2))) __bf16 bf16x2;
typedef __attribute__((ext_vector_type(4))) short s16x4;
typedef __attribute__((address_space(3))) char* lds_ptr;

constexpr int LDS_BYTES = 163840;

constexpr int WS_EPOCH_A = 0;
constexpr int WS_EPOCH_B = 128;
constexpr int WS_FLAGH = 4096;
constexpr int WS_FLAG1 = 65536;
constexpr int WS_FLAG2 = WS_FLAG1 + 65536;
constexpr int WS_FLAGB = WS_FLAG2 + 65536;
constexpr int WS_PART = 512 << 10;
constexpr int PART_STRIDE = 1792;
constexpr int WS_O = 2 << 20;
constexpr int WS_U = 5 << 20;
constexpr int WS_USED = 8 << 20;
constexpr int WS_STAMP = 16 << 20;

struct Params {
    const char* x; const char* wqkv; const char* wphi; const char* wo;
    const char* q; const char* k; const char* v; const char* shk; const char* hk; const char* dv;
    char* out; char* ws;
};

struct Stamps { unsigned c, w; };
#define STAMP_ARG , Stamps& ST
#define STAMP_PASS , ST
#define STAMP(n) do { SBAR(); u64 c_, w_; \
    asm volatile("s_memtime %0\n\ts_memrealtime %1\n\ts_waitcnt lgkmcnt(0)" : "=&s"(c_), "=&s"(w_) :: "memory"); \
    asm volatile("v_writelane_b32 %0, %2, " #n "\n\tv_writelane_b32 %1, %3, " #n : "+v"(ST.c), "+v"(ST.w) : "s"((unsigned)c_), "s"((unsigned)w_)); SBAR(); } while (0)

template <int V> struct ic { static constexpr int value = V; };
template <int B, int E, class Fn> __device__ __forceinline__ void static_for(Fn&& fn) {
    if constexpr (B < E) { fn(ic<B>{}); static_for<B + 1, E>(fn); }
}
#define SBAR() __builtin_amdgcn_sched_barrier(0)
#define FENCE() asm volatile("" ::: "memory")

__device__ __forceinline__ void dma16(const char* sbase, unsigned voff, unsigned lds_dst) {
    asm volatile("s_mov_b32 m0, %2\n\ts_nop 0\n\tglobal_load_lds_dwordx4 %0, %1" :: "v"(voff), "s"(sbase), "s"(lds_dst) : "memory");
}
__device__ __forceinline__ void set_m0(unsigned d) { asm volatile("s_mov_b32 m0, %0\n\ts_nop 0" :: "s"(d) : "memory"); }
__device__ __forceinline__ void dma_off(int off, const char* sbase, unsigned voff) {
    asm volatile("global_load_lds_dwordx4 %0, %1 offset:%2" :: "v"(voff), "s"(sbase), "i"(off) : "memory");
}
__device__ __forceinline__ void wait_vm_n(int n) { asm volatile("s_waitcnt vmcnt(%0)" :: "i"(n) : "memory"); }
__device__ __forceinline__ void bar_raw() { asm volatile("s_barrier" ::: "memory"); }
__device__ __forceinline__ lds_ptr opaque(lds_ptr p) { asm volatile("" : "+v"(p)); return p; }
template <int N> __device__ __forceinline__ void wait_vm() { asm volatile("s_waitcnt vmcnt(%0)" :: "i"(N) : "memory"); }
__device__ __forceinline__ void bar() { asm volatile("s_waitcnt lgkmcnt(0)\n\ts_barrier" ::: "memory"); }
__device__ __forceinline__ void touch(const char* sbase, unsigned voff, unsigned lds_dst) {
    asm volatile("s_mov_b32 m0, %2\n\ts_nop 0\n\tglobal_load_lds_dword %0, %1" :: "v"(voff), "s"(sbase), "s"(lds_dst) : "memory");
}
__device__ __forceinline__ u32x4 lds_r128(lds_ptr p) { return *(__attribute__((address_space(3))) u32x4*)p; }
__device__ __forceinline__ u32x2 lds_r64(lds_ptr p) { return *(__attribute__((address_space(3))) u32x2*)p; }
__device__ __forceinline__ unsigned lds_r32(lds_ptr p) { return *(__attribute__((address_space(3))) unsigned*)p; }
__device__ __forceinline__ void lds_w128(lds_ptr p, u32x4 v) { *(__attribute__((address_space(3))) u32x4*)p = v; }
__device__ __forceinline__ void lds_w64(lds_ptr p, u32x2 v) { *(__attribute__((address_space(3))) u32x2*)p = v; }
__device__ __forceinline__ void lds_w32(lds_ptr p, unsigned v) { *(__attribute__((address_space(3))) unsigned*)p = v; }
__device__ __forceinline__ u32x2 lds_tr(lds_ptr p) {
    return __builtin_bit_cast(u32x2, __builtin_amdgcn_ds_read_tr16_b64_v4i16((__attribute__((address_space(3))) s16x4*)p));
}
__device__ __forceinline__ u32x4 cat(u32x2 a, u32x2 b) { return u32x4{a.x, a.y, b.x, b.y}; }
__device__ __forceinline__ f32x4 mfma32(u32x4 a, u32x4 b, f32x4 c) {
    return __builtin_amdgcn_mfma_f32_16x16x32_bf16(__builtin_bit_cast(bf16x8, a), __builtin_bit_cast(bf16x8, b), c, 0, 0, 0);
}
__device__ __forceinline__ f32x16 mfma_w(u32x4 a, u32x4 b, f32x16 c) {
    return __builtin_amdgcn_mfma_f32_32x32x16_bf16(__builtin_bit_cast(bf16x8, a), __builtin_bit_cast(bf16x8, b), c, 0, 0, 0);
}
__device__ __forceinline__ unsigned pk(float lo, float hi) {
    return __builtin_bit_cast(unsigned, __builtin_convertvector(f32x2{lo, hi}, bf16x2));
}
__device__ __forceinline__ float blo(unsigned x) { return __uint_as_float(x << 16); }
__device__ __forceinline__ float bhi(unsigned x) { return __uint_as_float(x & 0xffff0000u); }

__device__ __forceinline__ u64 ld_agent(const char* p) { return __hip_atomic_load((const u64*)p, __ATOMIC_RELAXED, __HIP_MEMORY_SCOPE_AGENT); }
__device__ __forceinline__ void st_agent(char* p, u64 v) { __hip_atomic_store((u64*)p, v, __ATOMIC_RELAXED, __HIP_MEMORY_SCOPE_AGENT); }

__device__ __forceinline__ void wait_flags(const char* flags, int n, int lane, u64 epoch) {
    const char* p = flags + (lane < n ? lane : 0) * 128;
    for (int spin = 0; spin < (1 << 22); ++spin) {
        u64 f = ld_agent(p);
        if (__all(f == epoch)) break;
        __builtin_amdgcn_s_sleep(1);
    }
    FENCE();
}

#define O_AHEAD 12
#define Q_S1 8
#define P_AHEAD 20
#define MX3(a, b, c) __builtin_fmaxf(__builtin_fmaxf((a), (b)), (c))
#define MN3(a, b, c) __builtin_fminf(__builtin_fminf((a), (b)), (c))

__device__ __forceinline__ void dual_softmax(const float (&p)[20], unsigned (&F)[20]) {
    const float x0 = MX3(p[0], p[1], p[2]), x1 = MX3(p[3], p[4], p[5]), x2 = MX3(p[6], p[7], p[8]), x3 = MX3(p[9], p[10], p[11]);
    const float n0 = MN3(p[0], p[1], p[2]), n1 = MN3(p[3], p[4], p[5]), n2 = MN3(p[6], p[7], p[8]), n3 = MN3(p[9], p[10], p[11]);
    const float x4 = MX3(x0, p[12], p[13]), x5 = MX3(x1, p[14], p[15]), x6 = MX3(x2, p[16], p[17]), x7 = MX3(x3, p[18], p[19]);
    const float n4 = MN3(n0, p[12], p[13]), n5 = MN3(n1, p[14], p[15]), n6 = MN3(n2, p[16], p[17]), n7 = MN3(n3, p[18], p[19]);
    constexpr float L2E = 1.4426950408889634f;
    const float cp = __builtin_fmaxf(MX3(x4, x5, x6), x7) * -L2E, cn = __builtin_fminf(MN3(n4, n5, n6), n7) * L2E;
    f32x2 ep[10], en[10];
#pragma unroll
    for (int i = 0; i < 10; ++i) {
        const f32x2 v = {p[2 * i], p[2 * i + 1]};
        ep[i] = __builtin_elementwise_fma(v, f32x2{L2E, L2E}, f32x2{cp, cp});
        en[i] = __builtin_elementwise_fma(v, f32x2{-L2E, -L2E}, f32x2{cn, cn});
    }
    SBAR();
#pragma unroll
    for (int i = 0; i < 10; ++i) ep[i] = f32x2{__builtin_amdgcn_exp2f(ep[i].x), __builtin_amdgcn_exp2f(ep[i].y)};
    SBAR();
    const f32x2 sp = ((ep[0] + ep[1]) + (ep[2] + ep[3])) + ((ep[4] + ep[5]) + (ep[6] + ep[7])) + (ep[8] + ep[9]);
#pragma unroll
    for (int i = 0; i < 10; ++i) en[i] = f32x2{__builtin_amdgcn_exp2f(en[i].x), __builtin_amdgcn_exp2f(en[i].y)};
    const float rp = __builtin_amdgcn_rcpf(sp.x + sp.y);
    SBAR();
    const f32x2 sn = ((en[0] + en[1]) + (en[2] + en[3])) + ((en[4] + en[5]) + (en[6] + en[7])) + (en[8] + en[9]);
#pragma unroll
    for (int i = 0; i < 10; ++i) ep[i] *= rp;
    const float rn = __builtin_amdgcn_rcpf(sn.x + sn.y);
    SBAR();
#pragma unroll
    for (int i = 0; i < 10; ++i) F[i] = pk(ep[i].x, ep[i].y);
    SBAR();
#pragma unroll
    for (int i = 0; i < 10; ++i) en[i] *= rn;
    SBAR();
#pragma unroll
    for (int i = 0; i < 10; ++i) F[10 + i] = pk(en[i].x, en[i].y);
    SBAR();
}

__device__ __forceinline__ void project(const unsigned (&X)[2][10], const u32x4 (&W)[11], float (&p)[2][20]) {
    const f32x16 z = {};
    const u32x4 xr = {X[0][8], X[0][9], X[1][8], X[1][9]};
    u32x4 xf[2][2];
#pragma unroll
    for (int u = 0; u < 2; ++u)
#pragma unroll
        for (int c = 0; c < 2; ++c) xf[u][c] = u32x4{X[u][4 * c], X[u][4 * c + 1], X[u][4 * c + 2], X[u][4 * c + 3]};
    f32x16 p2 = mfma_w(W[6], xf[0][0], z);
    f32x16 p0 = mfma_w(W[0], xf[0][0], z);
    p2 = mfma_w(W[7], xf[0][1], p2);
    p0 = mfma_w(W[1], xf[0][1], p0);
    p2 = mfma_w(W[8], xf[1][0], p2);
    p2 = mfma_w(W[9], xf[1][1], p2);
    p2 = mfma_w(W[10], xr, p2);
    p0 = mfma_w(W[2], xr, p0);
    f32x16 p1 = mfma_w(W[3], xf[1][0], z);
    p1 = mfma_w(W[4], xf[1][1], p1);
    p1 = mfma_w(W[5], xr, p1);
#pragma unroll
    for (int e = 0; e < 16; ++e) { p[0][e] = p0[e]; p[1][e] = p1[e]; }
#pragma unroll
    for (int e = 0; e < 4; ++e) { p[0][16 + e] = p2[e]; p[1][16 + e] = p2[4 + e]; }
}

constexpr int A_WQ = 65536;
constexpr int wq_pos(int u) { return A_WQ + (u < 16 ? u + 2 : u - 16) * 5120; }
constexpr int wo_pos(int p) { return p < 4 ? p * 10240 : wq_pos(2) + (p - 4) * 10240; }
constexpr int A_OST = wq_pos(10);

__device__ __forceinline__ unsigned wo_voff(int lane) { return (lane >> 3) * 1280 + (((lane & 7) ^ ((lane >> 2) & 4)) * 16) + 4096; }
__device__ __forceinline__ void stage_wo(const Params& P, unsigned lds0, int ct, int rot, int p, unsigned voff) {
    const char* s = P.wo + ct * 128 + ((p + rot) & 7) * (80 * 1280);
    const unsigned d = lds0 + p * 10240 + (p >= 4 ? wo_pos(4) - 40960 : 0);
#pragma unroll
    for (int h = 0; h < 2; ++h) {
        set_m0(d + h * 5120 + 4096);
#pragma unroll
        for (int j = 0; j < 5; ++j) dma_off((j - 4) * 1024, s + h * 51200 + j * (10240 - 1024), voff);
    }
}

__device__ __forceinline__ void out_tile(const Params& P, lds_ptr L, u64 epoch, int r, int ct, int rot, int wv, int lane STAMP_ARG) {
    const __amdgpu_buffer_rsrc_t wsr = __builtin_amdgcn_make_buffer_rsrc((void*)P.ws, 0, WS_USED, 0x00020000);
    const int hi = lane >> 5, g1 = (lane >> 4) & 1, tq = (lane & 15) >> 2, tp = lane & 3;
    lds_ptr wb[2][2];
    const int wo_ = (hi * 20 + tq) * 128 + g1 * 32 + (tp & 1) * 16 + (tp >> 1) * 8;
#pragma unroll
    for (int h = 0; h < 2; ++h)
#pragma unroll
        for (int t = 0; t < 2; ++t) wb[h][t] = opaque(L + wo_pos(4 * h) + wo_ + ((t ^ (tq >> 1)) * 64));
    u32x4 wf[2][2], of[40];
    auto rd = [&](int s, int t) {
        const int p = s / 5, e = s % 5, h = p >> 2;
        lds_ptr b = wb[h][t] + (p & 3) * 10240;
        if (e < 4) wf[s & 1][t] = cat(lds_tr(b + ((e >> 1) * 40 + (e & 1) * 8) * 128), lds_tr(b + ((e >> 1) * 40 + (e & 1) * 8 + 4) * 128));
        else wf[s & 1][t] = cat(lds_tr(b + 16 * 128), lds_tr(b + 56 * 128));
    };
    rd(0, 0); rd(0, 1);
    wait_flags(P.ws + WS_FLAG2 + (r * 4 + wv) * 8 * 128, 8, lane, epoch);
    STAMP(11);
    auto ld = [&](int s) {
        const int p = s / 5, e = s % 5;
        of[s] = __builtin_amdgcn_raw_buffer_load_b128(wsr, lane * 16 + (e & 3) * 1024, WS_O + ((r * 4 + wv) * 8 + ((p + rot) & 7)) * 5120 + (e >> 2) * 4096, 16);
    };
#pragma unroll
    for (int s = 0; s < O_AHEAD; ++s) ld(s);
    SBAR();
    f32x16 acc[2] = {};
#pragma unroll
    for (int s = 0; s < 40; ++s)
#pragma unroll
        for (int t = 0; t < 2; ++t) {
            if (s < 39) rd(s + 1, t);
            acc[t] = mfma_w(wf[s & 1][t], of[s], acc[t]);
            if (t == 0 && s + O_AHEAD < 40) ld(s + O_AHEAD);
            SBAR();
        }
    STAMP(12);
    lds_ptr st = L + A_OST + wv * 4096;
    const int tok = lane & 31;
#pragma unroll
    for (int c = 0; c < 4; ++c) {
        const f32x16& a = acc[c >> 1];
        const int e = (c & 1) * 8;
        lds_w128(st + tok * 128 + (((2 * c + hi) ^ (tok & 7)) * 16),
                 u32x4{pk(a[e], a[e + 1]), pk(a[e + 2], a[e + 3]), pk(a[e + 4], a[e + 5]), pk(a[e + 6], a[e + 7])});
    }
    FENCE();
    char* ob = P.out + (size_t)(r * 128 + wv * 32 + (lane >> 3)) * 2560 + ct * 128 + (lane & 7) * 16;
#pragma unroll
    for (int j = 0; j < 4; ++j) *(u32x4*)(ob + j * 8 * 2560) = lds_r128(st + j * 1024 + (lane >> 3) * 128 + (((lane & 7) ^ (lane >> 3)) * 16));
    STAMP(13);
}

constexpr int A_WV = 10368, A_X = 20608, A_SLOT = 36992;
constexpr int A_PHI = 0, A_V = 40960;
constexpr int A_WPHI = 157696, A_ZERO = A_WPHI + 3200, A_EPOCH = A_ZERO + 2304;

__device__ __forceinline__ void load_wphi(lds_ptr L, int lane, int t, u32x4 (&wp)[11], lds_ptr pb0, lds_ptr pb1, lds_ptr pb2) {
    const u32x2 z2 = {0u, 0u};
    if (t < 2) {
        wp[3 * t] = cat(lds_tr(pb0 + t * 1600), lds_tr(pb0 + t * 1600 + 160));
        wp[3 * t + 1] = cat(lds_tr(pb0 + t * 1600 + 320), lds_tr(pb0 + t * 1600 + 480));
        wp[3 * t + 2] = t ? cat(z2, lds_tr(pb0 + 1600 + 640)) : cat(lds_tr(pb0 + 640), z2);
    } else if (t < 4) {
        lds_ptr b = t == 2 ? pb1 : pb2;
        wp[2 + 2 * t] = cat(lds_tr(b), lds_tr(b + 160));
        wp[3 + 2 * t] = cat(lds_tr(b + 320), lds_tr(b + 480));
    } else wp[10] = cat(lds_tr(pb1 + 640), lds_tr(pb2 + 640));
}
#define WPHI_BASES() \
    const bool act = hi == (tp & 1); const int gam = 2 * g1 + (tp >> 1); \
    lds_ptr pb0 = opaque(L + (act ? A_WPHI + hi * 800 + tq * 40 + gam * 8 : A_ZERO)); \
    lds_ptr pb1 = opaque(L + (act && gam == 0 ? A_WPHI + hi * 800 + tq * 40 + 32 : A_ZERO)); \
    lds_ptr pb2 = opaque(L + (act && gam == 1 ? A_WPHI + (2 + hi) * 800 + tq * 40 + 32 : A_ZERO))

__device__ __forceinline__ u64 role_k(const Params& P, lds_ptr L, int r, int g, int ct, int rot, int wv STAMP_ARG) {
    const int tid = threadIdx.x, lane = tid & 63;
    const unsigned lds0 = (unsigned)(uintptr_t)L;
    const int n32 = lane & 31, hi = lane >> 5, g1 = (lane >> 4) & 1, tq = (lane & 15) >> 2, tp = lane & 3;
    {
        const char* xsb = P.x + (r * 128 + wv * 32) * 1280;
        set_m0(lds0 + A_X + wv * 4096);
#pragma unroll
        for (int j = 0; j < 4; ++j) dma_off(j * 1024, xsb + j * (8 * 1280 - 1024), (lane >> 3) * 1280 + (((lane & 7) ^ (4 * (j & 1) + (lane >> 4))) * 16));
    }
    if (tid < 200) dma16(P.wphi + g * 3200 + wv * 1024, lane * 16, lds0 + A_WPHI + wv * 1024);
    u64 epoch = ld_agent(P.ws + WS_EPOCH_A);
    __builtin_amdgcn_s_setprio(2);
    if (tid < 144) lds_w128(L + A_ZERO + tid * 16, u32x4{0, 0, 0, 0});

    lds_ptr xa[2][4], wa[2];
#pragma unroll
    for (int h = 0; h < 2; ++h) {
#pragma unroll
        for (int ks = 0; ks < 4; ++ks) xa[h][ks] = opaque(L + h * 2 * A_SLOT + (wv * 32 + n32) * 128 + (((2 * ks + hi) ^ ((n32 >> 1) & 7)) * 16));
        wa[h] = opaque(L + h * 2 * A_SLOT + g1 * A_WV + (8 * hi + tq) * 160 + tp * 40);
    }
    f32x16 acc[5] = {};
    u32x4 xf[2], wf[2][5], wp[11];
    auto rd_main = [&](int s, int t) {
        const int kb = s >> 2, ks = s & 3, h = (kb >> 1) & 1, o = (kb & 1) * A_SLOT;
        if (t == 0) xf[s & 1] = lds_r128(xa[h][ks] + o + A_X);
        wf[s & 1][t] = cat(lds_tr(wa[h] + o + ks * 2560 + t * 8), lds_tr(wa[h] + o + ks * 2560 + 640 + t * 8));
    };
    WPHI_BASES();
    STAMP(1);
    wait_vm<2>();
    bar();
    STAMP(2);
#pragma unroll
    for (int t = 0; t < 5; ++t) rd_main(0, t);
    SBAR();
    static_for<0, 200>([&](auto G) {
        constexpr int gap = G.value, s = gap / 5, t = gap % 5, kb = s >> 2;
        if constexpr ((s & 3) == 3 && t == 0) {
            if constexpr (kb == 2) wait_vm<0>();
            if constexpr (kb < 7 || kb == 8) bar_raw(); else bar();
        }
        if constexpr (s < 39) rd_main(s + 1, t);
        else load_wphi(L, lane, t, wp, pb0, pb1, pb2);
        acc[t] = mfma_w(wf[s & 1][t], xf[s & 1], acc[t]);
        SBAR();
    });
    STAMP(3);
    epoch += 1;
    {
        unsigned X[2][10], F[20];
        float p[2][20];
#pragma unroll
        for (int u = 0; u < 2; ++u)
#pragma unroll
            for (int t = 0; t < 5; ++t) { X[u][2 * t] = pk(acc[t][4 * u], acc[t][4 * u + 1]); X[u][2 * t + 1] = pk(acc[t][4 * u + 2], acc[t][4 * u + 3]); }
        project(X, wp, p);
        const int token = wv * 32 + n32;
        lds_ptr vp = L + A_V + hi * 6144 + token * 48;
#pragma unroll
        for (int u = 0; u < 2; ++u) {
            unsigned V[10];
#pragma unroll
            for (int t = 0; t < 5; ++t) { V[2 * t] = pk(acc[t][8 + 4 * u], acc[t][9 + 4 * u]); V[2 * t + 1] = pk(acc[t][10 + 4 * u], acc[t][11 + 4 * u]); }
            lds_w128(vp + u * 12288, u32x4{V[0], V[1], V[2], V[3]});
            lds_w128(vp + u * 12288 + 16, u32x4{V[4], V[5], V[6], V[7]});
            lds_w128(vp + u * 12288 + 32, u32x4{V[8], V[9], 0x00003f80u, 0u});
        }
        if (tid == 0) lds_w64(L + A_EPOCH, u32x2{(unsigned)epoch, (unsigned)(epoch >> 32)});
        SBAR();
        STAMP(17);
        lds_ptr pp = L + A_PHI + hi * 10240 + token * 80;
#pragma unroll
        for (int u = 0; u < 2; ++u) {
            dual_softmax(p[u], F);
#pragma unroll
            for (int c = 0; c < 5; ++c) lds_w128(pp + u * 20480 + c * 16, u32x4{F[4 * c], F[4 * c + 1], F[4 * c + 2], F[4 * c + 3]});
        }
    }
    const __amdgpu_buffer_rsrc_t wsr = __builtin_amdgcn_make_buffer_rsrc((void*)P.ws, 0, WS_USED, 0x00020000);
    const int set = (g * 4 + wv) * 16;
    {
        const int tok = lane & 15, gq = lane >> 4;
        const int trow = 16 * hi + 8 * g1 + 2 * tq;
        lds_ptr pa = opaque(L + A_PHI + wv * 10240 + trow * 80 + tp * 8);
        lds_ptr pb = opaque(L + A_V + wv * 6144 + trow * 48 + tp * 8);
        const int base = WS_PART + (set + r) * PART_STRIDE;
        int o4a = base + lane * 16;
        int o2a = hi == 0 ? base + 1024 + lane * 8 : WS_USED;
        int o4b = tok < 5 ? base + 1280 + (gq * 5 + tok) * 16 : WS_USED;
        int o2b = (tok < 5 && hi == 0) ? base + 1600 + (gq * 5 + tok) * 8 : WS_USED;
        asm volatile("" : "+v"(o4a), "+v"(o2a), "+v"(o4b), "+v"(o2b));
        STAMP(18);
        bar();
        STAMP(4);
        u32x4 vb[4][2], pf[4][3];
        auto rd = [&](int ks) {
#pragma unroll
            for (int d = 0; d < 2; ++d) vb[ks][d] = cat(lds_tr(pb + ks * 1536 + d * 32), lds_tr(pb + ks * 1536 + 48 + d * 32));
#pragma unroll
            for (int f = 0; f < 3; ++f) pf[ks][f] = cat(lds_tr(pa + ks * 2560 + f * 32), lds_tr(pa + ks * 2560 + 80 + f * 32));
        };
        rd(0);
        SBAR();
        f32x4 kv[2][3] = {};
#pragma unroll
        for (int ks = 0; ks < 4; ++ks) {
            if (ks < 3) rd(ks + 1);
            SBAR();
#pragma unroll
            for (int f = 0; f < 3; ++f)
#pragma unroll
                for (int d = 0; d < 2; ++d) kv[d][f] = mfma32(pf[ks][f], vb[ks][d], kv[d][f]);
            SBAR();
        }
#pragma unroll
        for (int d = 0; d < 2; ++d) {
            const u32x4 s4 = {pk(kv[d][0][0], kv[d][0][1]), pk(kv[d][0][2], kv[d][0][3]), pk(kv[d][1][0], kv[d][1][1]), pk(kv[d][1][2], kv[d][1][3])};
            const u32x2 s2 = {pk(kv[d][2][0], kv[d][2][1]), pk(kv[d][2][2], kv[d][2][3])};
            __builtin_amdgcn_raw_buffer_store_b128(s4, wsr, d ? o4b : o4a, 0, 16);
            __builtin_amdgcn_raw_buffer_store_b64(s2, wsr, d ? o2b : o2a, 0, 16);
        }
    }
    STAMP(5);
    const unsigned wov = wo_voff(lane);
    stage_wo(P, lds0, ct, rot, wv, wov);
    wait_vm<10>();
    if (lane == 0) st_agent(P.ws + WS_FLAG1 + (set + r) * 128, epoch);
    STAMP(6);
    lds_ptr kw[2][2];
    {
        const int a0 = (lane & 15) * 80 + (lane >> 4) * 8;
        int b0, b1;
        if (lane < 16) { b0 = ((2 * lane) & 15) * 80 + 64 + (lane >> 3) * 8; b1 = b0 + 80; }
        else if (lane < 36) { const int e = lane - 16, gg = e / 5; b0 = (16 + e - gg * 5) * 80 + gg * 8; b1 = b0 + 32; }
        else if (lane < 41) { const int e = 2 * (lane - 36), g0 = e / 5, g2 = (e + 1) / 5; b0 = (16 + e - g0 * 5) * 80 + 64 + g0 * 8; b1 = (17 + e - g2 * 5) * 80 + 64 + g2 * 8; }
        else { b0 = 2048 + lane * 16; b1 = b0 + 8; }
        lds_ptr kb = L + A_V + wv * 6144;
        kw[0][0] = opaque(kb + a0); kw[0][1] = opaque(kb + a0 + 32); kw[1][0] = opaque(kb + b0); kw[1][1] = opaque(kb + b1);
    }
    u32x4 sel[2];
    {
        const int sj = (n32 & 3) + 4 * ((n32 >> 3) & 1);
        const unsigned one = (((n32 >> 2) & 1) == hi) ? ((sj & 1) ? 0x3f800000u : 0x00003f80u) : 0u;
#pragma unroll
        for (int m = 0; m < 2; ++m)
#pragma unroll
            for (int e = 0; e < 4; ++e) sel[m][e] = (g1 == m && (sj >> 1) == e) ? one : 0u;
        asm volatile("" : "+v"(sel[0]), "+v"(sel[1]));
    }
    wait_vm<0>();
    STAMP(7);
    wait_flags(P.ws + WS_FLAG1 + set * 128, 16, lane, epoch);
    STAMP(8);
    {
        u32x4 pr[32];
        const int uo0 = lane * 16, uo1 = lane < 41 ? 1024 + lane * 16 : WS_USED;
        auto ld = [&](int e) {
            pr[e] = __builtin_amdgcn_raw_buffer_load_b128(wsr, (e & 1) ? uo1 : uo0, WS_PART + (set + (((e >> 1) + 4 * (r & 3)) & 15)) * PART_STRIDE, 16);
        };
#pragma unroll
        for (int e = 0; e < P_AHEAD; ++e) ld(e);
        SBAR();
        f32x16 s = {};
#pragma unroll
        for (int e = 0; e < 32; ++e) {
            s = mfma_w(sel[e & 1], pr[e], s);
            if (e + P_AHEAD < 32) ld(e + P_AHEAD);
            SBAR();
        }
#pragma unroll
        for (int m = 0; m < 2; ++m) {
            lds_w64(kw[m][0], u32x2{pk(s[8 * m], s[8 * m + 1]), pk(s[8 * m + 2], s[8 * m + 3])});
            lds_w64(kw[m][1], u32x2{pk(s[8 * m + 4], s[8 * m + 5]), pk(s[8 * m + 6], s[8 * m + 7])});
        }
    }
    STAMP(19);
    bar();
    __builtin_amdgcn_s_setprio(0);
    STAMP(9);
    stage_wo(P, lds0, ct, rot, 4 + wv, wov);
    if (r == 0 && g == 0 && wv == 0) wait_flags(P.ws + WS_FLAGH, 32, lane, epoch);
    wait_vm<0>();
    bar_raw();
    return epoch;
}

__device__ __forceinline__ void role_q(const Params& P, lds_ptr L, int r, int g, int wv STAMP_ARG) {
    const int lane = threadIdx.x & 63;
    const unsigned lds0 = (unsigned)(uintptr_t)L;
    const int n32 = lane & 31, hi = lane >> 5, g1 = (lane >> 4) & 1, tq = (lane & 15) >> 2, tp = lane & 3;
    unsigned xo[2], wo5[5];
#pragma unroll
    for (int j = 0; j < 5; ++j) wo5[j] = (((j * 64 + lane) * 6554) >> 16) * (3840 - 160) + lane * 16 + 1024;
    const char* xsb = P.x + (r * 128 + wv * 32) * 1280;
    const char* wsb = P.wqkv + 1280 * (1 + (wv & 1)) + g * 160 + (wv >> 1) * (32 * 3840);
    const char* wqb = P.wqkv + g * 160 + wv * (32 * 3840);
    auto stage_w = [&](const char* s, unsigned d) {
        set_m0(d + 1024);
#pragma unroll
        for (int j = 0; j < 5; ++j) dma_off((j - 1) * 1024, s, wo5[j]);
    };
    auto stage = [&](int n) {
        stage_w(wsb + n * (64 * 3840), lds0 + (wv & 1) * A_WV + (wv >> 1) * 5120 + (n & 3) * A_SLOT);
        if (n > 0) {
            set_m0(lds0 + A_X + wv * 4096 + (n & 3) * A_SLOT);
#pragma unroll
            for (int j = 0; j < 4; ++j) dma_off(j * 1024, xsb + n * 128 + j * (8 * 1280 - 1024), xo[j & 1]);
        }
    };
    stage(0);
#pragma unroll
    for (int j = 0; j < 2; ++j) xo[j] = (lane >> 3) * 1280 + (((lane & 7) ^ (4 * j + (lane >> 4))) * 16);
    stage(1);
    u32x4 xk[40];
    {
        lds_ptr xa[4];
#pragma unroll
        for (int ks = 0; ks < 4; ++ks) xa[ks] = opaque(L + A_X + (wv * 32 + n32) * 128 + (((2 * ks + hi) ^ ((n32 >> 1) & 7)) * 16));
        STAMP(1);
        wait_vm<9>();
        bar();
        STAMP(2);
#pragma unroll
        for (int n = 0; n < 10; ++n) {
#pragma unroll
            for (int ks = 0; ks < 4; ++ks) xk[n * 4 + ks] = lds_r128(xa[ks] + (n & 3) * A_SLOT);
            if (n < 8) { stage(n + 2); wait_vm<9>(); }
            else {
                stage_w(wqb + (n - 8) * 4 * (32 * 3840), lds0 + wq_pos((n - 8) * 4) + wv * 5120);
                wait_vm<5>();
            }
            if (n == 2) STAMP(21);
            if (n == 5) STAMP(22);
            if (n == 8) STAMP(23);
            bar();
        }
    }
    STAMP(3);
#pragma unroll
    for (int h = 2; h < 4; ++h) stage_w(wqb + h * 4 * (32 * 3840), lds0 + wq_pos(h * 4) + wv * 5120);
    if (wv < 2) stage_w(wqb + 16 * (32 * 3840), lds0 + wq_pos(16) + wv * 5120);
    f32x16 acc[3] = {};
    u32x4 wp[11];
    {
        u32x4 wf[2][3];
        lds_ptr qa[2];
#pragma unroll
        for (int h = 0; h < 2; ++h) qa[h] = opaque(L + A_WQ + h * 46080 + (8 * hi + tq) * 160 + tp * 40 + g1 * 8);
        auto rd_q = [&](int s, int t) {
            const int o = wq_pos(s >> 1) - A_WQ, h = o >= 46080;
            lds_ptr b = qa[h] + o - h * 46080 + (s & 1) * 2560 + t * 16;
            wf[s & 1][t] = cat(lds_tr(b), lds_tr(b + 640));
        };
#pragma unroll
        for (int t = 0; t < 3; ++t) rd_q(0, t);
        SBAR();
        static_for<0, 40>([&](auto S) {
            constexpr int s = S.value;
            if constexpr (s == Q_S1 - 1) {
                STAMP(18);
                wait_vm<0>();
                bar();
                STAMP(4);
            }
            if constexpr (s >= Q_S1 && s < Q_S1 + 10) {
                constexpr int e = s - Q_S1;
                if constexpr (e % 5 == 0) set_m0(lds0 + wq_pos(18 + e / 5) + 1024);
                dma_off((e % 5 - 1) * 1024, P.wqkv + g * 160 + (18 + e / 5) * (32 * 3840), wo5[e % 5]);
            }
            if constexpr (s == 35) wait_vm<0>();
#pragma unroll
            for (int t = 0; t < 3; ++t) {
                if constexpr (s < 39) rd_q(s + 1, t);
                acc[t] = mfma_w(wf[s & 1][t], xk[s], acc[t]);
                SBAR();
            }
            if constexpr (s == 36) {
                WPHI_BASES();
#pragma unroll
                for (int t = 0; t < 5; ++t) load_wphi(L, lane, t, wp, pb0, pb1, pb2);
                SBAR();
            }
        });
    }
    STAMP(7);
    unsigned F[2][20];
    {
        unsigned X[2][10];
        float p[2][20];
#pragma unroll
        for (int u = 0; u < 2; ++u)
#pragma unroll
            for (int c = 0; c < 5; ++c) {
                const int e = 4 * (2 * (c & 1) + u);
                X[u][2 * c] = pk(acc[c >> 1][e], acc[c >> 1][e + 1]); X[u][2 * c + 1] = pk(acc[c >> 1][e + 2], acc[c >> 1][e + 3]);
            }
        project(X, wp, p);
        SBAR();
        dual_softmax(p[0], F[0]);
        dual_softmax(p[1], F[1]);
    }
    const u32x2 ev = lds_r64(L + A_EPOCH);
    const __amdgpu_buffer_rsrc_t wsr = __builtin_amdgcn_make_buffer_rsrc((void*)P.ws, 0, WS_USED, 0x00020000);
    const bool on = hi == ((n32 >> 2) & 1);
    const int gm = n32 >> 3, ii = n32 & 3;
    lds_ptr ka[2], kb[2];
#pragma unroll
    for (int u = 0; u < 2; ++u) {
        ka[u] = opaque(L + (on ? A_V + (2 * u + hi) * 6144 + (4 * gm + ii) * 80 : A_ZERO));
        kb[u] = opaque(L + (on && (gm >> 1) == u ? A_V + (2 * u + hi) * 6144 + (16 + 4 * (gm & 1) + ii) * 80 : A_ZERO));
    }
    int ob = WS_O + ((r * 4 + wv) * 8 + g) * 5120 + lane * 16;
    asm volatile("" : "+v"(ob));
    STAMP(14);
    bar();
    STAMP(9);
    u32x4 kf[20];
    auto rd = [&](int e) { kf[e] = lds_r128((e < 10 ? kb[e / 5] : ka[(e - 10) / 5]) + (e % 5) * 16); };
#pragma unroll
    for (int e = 0; e < 6; ++e) rd(e);
    SBAR();
    f32x16 n[3] = {};
    float rd0, rd1;
#pragma unroll
    for (int e = 0; e < 20; ++e) {
        const int u = (e / 5) & 1, f = e % 5, d = e < 10 ? 2 : u;
        if (e + 6 < 20) rd(e + 6);
        n[d] = mfma_w(kf[e], u32x4{F[u][4 * f], F[u][4 * f + 1], F[u][4 * f + 2], F[u][4 * f + 3]}, n[d]);
        if (e == 12) { rd0 = __builtin_amdgcn_rcpf(n[2][4] + 1e-6f); rd1 = __builtin_amdgcn_rcpf(n[2][12] + 1e-6f); }
        if (e == 14)
            __builtin_amdgcn_raw_buffer_store_b128(u32x4{pk(n[2][0] * rd0, n[2][1] * rd0), pk(n[2][2] * rd0, n[2][3] * rd0),
                                                         pk(n[2][8] * rd1, n[2][9] * rd1), pk(n[2][10] * rd1, n[2][11] * rd1)}, wsr, ob + 4096, 0, 16);
        SBAR();
    }
    STAMP(16);
#pragma unroll
    for (int c = 0; c < 4; ++c) {
        const f32x16& a = n[c >> 1];
        const int e = (c & 1) * 8;
        const float rr = (c >> 1) ? rd1 : rd0;
        __builtin_amdgcn_raw_buffer_store_b128(u32x4{pk(a[e] * rr, a[e + 1] * rr), pk(a[e + 2] * rr, a[e + 3] * rr),
                                                     pk(a[e + 4] * rr, a[e + 5] * rr), pk(a[e + 6] * rr, a[e + 7] * rr)}, wsr, ob + c * 1024, 0, 16);
    }
    STAMP(15);
    wait_vm<0>();
    if (lane == 0) st_agent(P.ws + WS_FLAG2 + ((r * 4 + wv) * 8 + g) * 128, ((u64)ev.y << 32) | ev.x);
    STAMP(10);
    wait_vm<0>();
    bar_raw();
}

#define PF_B 8
__device__ __forceinline__ void touch_kv(const Params& P, unsigned dst, int xcd, int q, int lane) {
    const int kb = (q * 3641) >> 16, s = q - kb * 18;
    const bool isx = s < 8;
    const int off = isx ? ((xcd >> 1) * 512 + s * 64) * 1280 + kb * 128
                        : kb * (64 * 3840) + 1280 + (xcd & 1) * 640 + (s - 8) * 128 + (s >= 13 ? 640 : 0);
    touch((isx ? P.x : P.wqkv) + off, lane * (isx ? 1280 : 3840), dst);
}

__device__ __forceinline__ u64 role_h(const Params& P, lds_ptr L, int xcd, int n, int ct, int wv STAMP_ARG) {
    const int lane = threadIdx.x & 63;
    const unsigned lds0 = (unsigned)(uintptr_t)L, dst = lds0 + LDS_BYTES - 256;
    const int id = wv * 6 + n;
    for (int q = PF_B * 10; q < 180; q += 48) {
        if (q + id < 180) touch_kv(P, dst, xcd, q + id, lane);
        bar_raw();
    }
    STAMP(1);
#pragma unroll
    for (int m = 0; m < 2; ++m) {
        const int rs = m ? 1280 : 3840;
        for (int e = id; e < 50; e += 48) {
            const int kb = (e * 13108) >> 16;
            touch((m ? P.wo : P.wqkv) + kb * 64 * rs + (xcd & 1) * 640 + (e - kb * 5) * 128, lane * rs, dst);
        }
        bar_raw();
    }
    for (int e = id; e < 64; e += 48) touch(P.q + (e >> 1) * (64 * 1280) + ((xcd * 160) & ~127) + (e & 1) * 128, lane * 1280, dst);
    STAMP(3);
    if (n >= 4) return 0;
    const u64 epoch = ld_agent(P.ws + WS_EPOCH_A) + 1;
    stage_wo(P, lds0, ct, 1, wv, wo_voff(lane));
    wait_vm<0>();
    bar();
    if (threadIdx.x == 0) st_agent(P.ws + WS_FLAGH + (xcd * 4 + n) * 128, epoch);
    STAMP(2);
    return epoch;
}

constexpr int B_HS = 8512, B_H1 = 64, B_H0 = 64 + 4096 + 132;
constexpr int B_R = 8 * B_HS, B_RS = 9152, B_R0 = 4576;
constexpr int B_K = B_R, B_V = B_R + 33 * 1032;

__device__ __forceinline__ void role_b(const Params& P, lds_ptr L, int xcd, int n, int wv STAMP_ARG) {
    const int tid = threadIdx.x, lane = tid & 63;
    const int grp = xcd * 10 + n, col = grp >> 3, j8 = grp & 7;
    const unsigned lds0 = (unsigned)(uintptr_t)L;
    if (wv < PF_B) touch_kv(P, lds0 + LDS_BYTES - 256, xcd, wv * 10 + n, lane);
    bar_raw();

    {
        const unsigned so = (lane >> 3) * 1280 + (lane & 7) * 16;
        const size_t tb = (size_t)(j8 * 256 + wv * 32) * 1280 + col * 128;
        if (wv == 0) {
            if (j8 > 0) dma16(P.k + tb - 8 * 1280, so, lds0 + B_K);
            else { lds_w64(L + B_K + lane * 16, u32x2{0, 0}); lds_w64(L + B_K + lane * 16 + 8, u32x2{0, 0}); }
        }
        set_m0(lds0 + B_K + (wv * 4 + 1) * 1032);
#pragma unroll
        for (int m = 0; m < 4; ++m) dma_off(m * 1032, P.k + tb + m * (10240 - 1032), so);
        set_m0(lds0 + B_V + wv * 4 * 1032);
#pragma unroll
        for (int m = 0; m < 4; ++m) dma_off(m * 1032, P.v + tb + m * (10240 - 1032), so);
    }
    const int ch = grp * 8 + wv;
    const int tau = lane & 31, cq = wv * 2 + (lane >> 5);
    const u32x4 sw0 = *(const u32x4*)(P.shk + (col * 64 + cq * 4) * 8), sw1 = *(const u32x4*)(P.shk + (col * 64 + cq * 4) * 8 + 16);
    const u64 epoch = ld_agent(P.ws + WS_EPOCH_B + col * 128) + 1;
    const unsigned dval = *(const unsigned short*)(P.dv + ch * 2);
    if (lane < 16) { lds_w32(L + wv * B_HS + lane * 4, 0u); lds_w32(L + wv * B_HS + B_H0 - 64 + lane * 4, 0u); }
    wait_vm<0>();
#pragma unroll
    for (int h = 0; h < 2; ++h) {
        set_m0(lds0 + wv * B_HS + (h ? B_H0 : B_H1));
#pragma unroll
        for (int m = 0; m < 4; ++m) dma_off(m * 1024, P.hk + ch * 4096, lane * 16);
    }
    bar();
    STAMP(1);
    const __amdgpu_buffer_rsrc_t wsr = __builtin_amdgcn_make_buffer_rsrc((void*)P.ws, 0, WS_USED, 0x00020000);
    {
        lds_ptr kp = opaque(L + B_K + tau * 1032 + cq * 8);
        u32x2 kr[11], vr[8];
#pragma unroll
        for (int e = 0; e < 11; ++e) { kr[e] = lds_r64(kp + (e < 3 ? (5 + e) * 128 : 1032 + (e - 3) * 128)); SBAR(); }
#pragma unroll
        for (int e = 0; e < 8; ++e) { vr[e] = lds_r64(kp + (B_V - B_K) + e * 128); SBAR(); }
        bar();
        f32x2 kk[11][2];
#pragma unroll
        for (int e = 0; e < 11; ++e) { kk[e][0] = f32x2{blo(kr[e].x), bhi(kr[e].x)}; kk[e][1] = f32x2{blo(kr[e].y), bhi(kr[e].y)}; }
        f32x2 w[2][4];
        w[0][0] = f32x2{blo(sw0.x), blo(sw0.z)}; w[0][1] = f32x2{bhi(sw0.x), bhi(sw0.z)};
        w[0][2] = f32x2{blo(sw0.y), blo(sw0.w)}; w[0][3] = f32x2{bhi(sw0.y), bhi(sw0.w)};
        w[1][0] = f32x2{blo(sw1.x), blo(sw1.z)}; w[1][1] = f32x2{bhi(sw1.x), bhi(sw1.z)};
        w[1][2] = f32x2{blo(sw1.y), blo(sw1.w)}; w[1][3] = f32x2{bhi(sw1.y), bhi(sw1.w)};
        f32x2 u[8][2];
#pragma unroll
        for (int e = 0; e < 8; ++e) {
            const u32x2 t = vr[e];
#pragma unroll
            for (int h = 0; h < 2; ++h) {
                f32x2 a = kk[e][h] * w[h][3];
                a = __builtin_elementwise_fma(kk[e + 1][h], w[h][2], a);
                a = __builtin_elementwise_fma(kk[e + 2][h], w[h][1], a);
                a = __builtin_elementwise_fma(kk[e + 3][h], w[h][0], a);
                const unsigned tv = h ? t.y : t.x;
                u[e][h] = a * f32x2{blo(tv), bhi(tv)};
            }
        }
        const int uo = (col * 64 + cq * 4) * 4096 + (255 - (j8 * 32 + tau)) * 16;
#pragma unroll
        for (int c = 0; c < 4; ++c) {
            const int h = c >> 1, e = c & 1;
            const u32x4 o = {pk(u[7][h][e], u[6][h][e]), pk(u[5][h][e], u[4][h][e]), pk(u[3][h][e], u[2][h][e]), pk(u[1][h][e], u[0][h][e])};
            __builtin_amdgcn_raw_buffer_store_b128(o, wsr, uo + WS_U + c * 4096, 0, 16);
        }
    }
    STAMP(2);
    lds_ptr R1 = L + B_R + wv * B_RS;
    if (lane < 28) { lds_w128(R1 + 4096 + lane * 16, u32x4{0, 0, 0, 0}); lds_w128(R1 + B_R0 + 4096 + lane * 16, u32x4{0, 0, 0, 0}); }
    wait_vm<0>();
    STAMP(3);
    if (lane == 0) st_agent(P.ws + WS_FLAGB + ((col * 8 + wv) * 8 + j8) * 128, epoch);
    wait_vm<0>();
    wait_flags(P.ws + WS_FLAGB + grp * 8 * 128, 8, lane, epoch);
    if (j8 == 0 && tid == 0) st_agent(P.ws + WS_EPOCH_B + col * 128, epoch);
    STAMP(4);
    {
        u32x4 a[4];
#pragma unroll
        for (int m = 0; m < 4; ++m) a[m] = __builtin_amdgcn_raw_buffer_load_b128(wsr, lane * 16 + m * 1024, WS_U + ch * 4096, 16);
#pragma unroll
        for (int m = 0; m < 4; ++m) lds_w128(R1 + lane * 16 + m * 1024, a[m]);
        FENCE();
#pragma unroll
        for (int m = 0; m < 4; ++m) {
            const unsigned nx = lds_r32(R1 + lane * 16 + m * 1024 + 16);
            const u32x4 s = {__builtin_amdgcn_alignbit(a[m].y, a[m].x, 16), __builtin_amdgcn_alignbit(a[m].z, a[m].y, 16),
                             __builtin_amdgcn_alignbit(a[m].w, a[m].z, 16), __builtin_amdgcn_alignbit(nx, a[m].w, 16)};
            lds_w128(R1 + B_R0 + lane * 16 + m * 1024, s);
        }
    }
    FENCE();
    STAMP(5);

    const int c16 = lane & 15, kq = lane >> 4;
    lds_ptr ub = R1 + ((c16 & 1) ? 0 : B_R0) + 448 - 64 * (c16 >> 1) + 16 * kq;
    lds_ptr hb = L + wv * B_HS + ((c16 & 1) ? B_H1 : B_H0) + 2 * (2 * c16 - 30 + 8 * kq);
    f32x4 y[8];
    u32x4 qv[4];
    {
        const int e = 30 - 2 * c16 - 8 * kq;
        const u32x4 hd = {e == 0 ? dval : 0u, e == 2 ? dval : 0u, e == 4 ? dval : 0u, e == 6 ? dval : 0u};
lds_ptr hb2 = opaque(hb + 8);
        u32x4 U[2][8], H[2][8];
        auto rd_c = [&](int rr, int s) {
            U[rr & 1][s] = lds_r128(ub + 512 * (7 - s) + 64 * rr);
            H[rr & 1][s] = cat(lds_r64(hb + 512 * s + 64 * rr), lds_r64(hb2 + 512 * s + 64 * rr));
        };
#pragma unroll
        for (int s = 0; s < 8; ++s) { rd_c(0, s); SBAR(); }
#pragma unroll
        for (int j = 0; j < 8; ++j) y[j] = mfma32(U[0][j], hd, f32x4{0.f, 0.f, 0.f, 0.f});
        SBAR();
#pragma unroll
        for (int rr = 0; rr < 8; ++rr) {
            if (rr < 4) qv[rr] = *(const u32x4*)(P.q + (size_t)(rr * 512 + tid) * 1280 + grp * 16);
            int cnt = 0;
#pragma unroll
            for (int s = 0; s < 8; ++s)
#pragma unroll
                for (int j = s; j < 8; ++j) {
                    if (rr < 7 && (cnt & 3) == 0 && cnt < 32) rd_c(rr + 1, cnt >> 2);
                    y[j] = mfma32(U[rr & 1][j - s], H[rr & 1][s], y[j]);
                    if ((cnt & 3) == 3) SBAR();
                    ++cnt;
                }
        }
    }
    STAMP(6);
    {
        lds_ptr yp = L + wv * B_HS + 4 * c16 + 128 * kq;
#pragma unroll
        for (int j = 0; j < 8; ++j) {
            lds_w32(yp + 512 * j, pk(y[j][0], y[j][1]));
            lds_w32(yp + 512 * j + 64, pk(y[j][2], y[j][3]));
        }
    }
    bar();
    STAMP(8);
    {
        lds_ptr yr = L + (c16 >> 2) * B_HS + 2 * (wv * 64 + kq * 16 + (lane & 3) * 4);
#pragma unroll
        for (int m = 0; m < 4; ++m) {
            const u32x4 yy = cat(lds_tr(yr + m * 1024), lds_tr(yr + 4 * B_HS + m * 1024));
            u32x4 o;
#pragma unroll
            for (int e = 0; e < 4; ++e) o[e] = pk(blo(yy[e]) * blo(qv[m][e]), bhi(yy[e]) * bhi(qv[m][e]));
            *(u32x4*)(P.out + (size_t)(m * 512 + tid) * 2560 + 1280 + grp * 16) = o;
        }
    }
    STAMP(7);
}

__host__ __device__ constexpr int role_of(int b) { return (b >> 3) < 16 ? 0 : ((b >> 3) < 26 ? 1 : ((b >> 3) < 30 ? 2 : 3)); }

__global__ void __launch_bounds__(512, 2) fused_kernel(Params P) {
    __shared__ __attribute__((aligned(16))) char smem[LDS_BYTES];
    lds_ptr L = (lds_ptr)smem;
    const int xcd = blockIdx.x & 7, i = blockIdx.x >> 3;
    const int wv = __builtin_amdgcn_readfirstlane(threadIdx.x >> 6);
    Stamps ST = {0u, 0u};
    STAMP(0);
    if (wv == 7) {
        const char* pc; unsigned len;
        asm volatile("s_getpc_b64 %0\nhk_pc:\n\ts_mov_b32 %1, hk_code_end - hk_pc\n\t.subsection 1\nhk_code_end:\n\t.subsection 0" : "=s"(pc), "=s"(len));
        const unsigned o = ((threadIdx.x & 63) * 32 + i) * 128;
        if (o < len && o < 49152) touch(pc, o, (unsigned)(uintptr_t)L + LDS_BYTES - 256);
    }
    asm volatile("" :: "s"(P.x), "s"(P.ws));
    STAMP(30);
    const bool is_a = i < 16;
    const int r = (xcd >> 1) * 4 + ((is_a ? i : i - 26) & 3), g = (xcd & 1) * 4 + (i >> 2);
    const int ct = (xcd & 1) * 5 + (is_a ? i >> 2 : 4), rot = is_a ? 2 * (i >> 2) : 1;
    u64 epoch = 0;
    bool out = false;
    if (is_a) {
        if (wv < 4) { epoch = role_k(P, L, r, g, ct, rot, wv STAMP_PASS); out = true; }
        else role_q(P, L, r, g, wv - 4 STAMP_PASS);
    } else if (i < 26) role_b(P, L, xcd, i - 16, wv STAMP_PASS);
    else { epoch = role_h(P, L, xcd, i - 26, ct, wv STAMP_PASS); out = i < 30 && wv < 4; }
    if (out) {
        out_tile(P, L, epoch, r, ct, rot, wv, threadIdx.x & 63 STAMP_PASS);
        if (blockIdx.x == 0 && threadIdx.x == 0) st_agent(P.ws + WS_EPOCH_A, epoch);
    }
    wait_vm<0>();
    STAMP(31);
    if ((threadIdx.x >> 6) == STAMPS && (threadIdx.x & 63) < 32) {
        ((long long*)(P.ws + WS_STAMP))[blockIdx.x * 32 + (threadIdx.x & 63)] = ST.w;
        ((long long*)(P.ws + WS_STAMP + 65536))[blockIdx.x * 32 + (threadIdx.x & 63)] = ST.c;
    }
}

}

__global__ void k_first(char* ws) { if (threadIdx.x == 0) ((long long*)(ws + (24 << 20)))[blockIdx.x] = (unsigned)wall_clock64(); }

__global__ void __launch_bounds__(256) probe_kernel(const char* ws, int nb) {
    const long long* s = (const long long*)(ws + hk::WS_STAMP);
    const long long* t = (const long long*)(ws + (24 << 20));
    long long mn = 1LL << 62, m0 = 1LL << 62;
    for (int i = 0; i < 256; ++i) mn = s[i * 32] < mn ? s[i * 32] : mn;
    for (int i = 0; i < nb; ++i) m0 = t[i] < m0 ? t[i] : m0;
    long long target = 10000 + ((mn - m0 + 5) / 10) * 2000;
    if (target > 3000000) target = 3000000;
    for (int spin = 0; spin < (1 << 24); ++spin) {
        const unsigned now = (unsigned)wall_clock64();
        if ((long long)(unsigned)(now - (unsigned)m0) >= target) break;
    }
}

extern "C" void kernel_launch(void* const* d_in, const int* in_sizes, int n_in,
                              void* d_out, int out_size, void* d_ws, size_t ws_size,
                              hipStream_t stream) {
    hk::Params P;
    P.x = (const char*)d_in[0]; P.wqkv = (const char*)d_in[1]; P.wphi = (const char*)d_in[2]; P.wo = (const char*)d_in[3];
    P.q = (const char*)d_in[4]; P.k = (const char*)d_in[5]; P.v = (const char*)d_in[6];
    P.shk = (const char*)d_in[7]; P.hk = (const char*)d_in[8]; P.dv = (const char*)d_in[9];
    P.out = (char*)d_out; P.ws = (char*)d_ws;
    k_first<<<1, 64, 0, stream>>>(P.ws);
    hk::fused_kernel<<<256, 512, 0, stream>>>(P);
    probe_kernel<<<1, 256, 0, stream>>>(P.ws, 1);
}
```

```cpp
#include <hip/hip_runtime.h>
#include <hip/hip_bf16.h>
#include <stdint.h>


namespace hk {

typedef unsigned long long u64;
typedef __attribute__((ext_vector_type(4))) unsigned u32x4;
typedef __attribute__((ext_vector_type(2))) unsigned u32x2;
typedef __attribute__((ext_vector_type(4))) float f32x4;
typedef __attribute__((ext_vector_type(16))) float f32x16;
typedef __attribute__((ext_vector_type(2))) float f32x2;
typedef __attribute__((ext_vector_type(8))) __bf16 bf16x8;
typedef __attribute__((ext_vector_type(2))) __bf16 bf16x2;
typedef __attribute__((ext_vector_type(4))) short s16x4;
typedef __attribute__((address_space(3))) char* lds_ptr;

constexpr int LDS_BYTES = 163840;

constexpr int WS_EPOCH_A = 0;
constexpr int WS_EPOCH_B = 128;
constexpr int WS_FLAGH = 4096;
constexpr int WS_FLAGP = 8192;
constexpr int WS_FLAGO = 10240;
constexpr int WS_FLAGU = 12288;
constexpr int WS_FLAGB = 65536;
constexpr int WS_PART = 512 << 10;
constexpr int PART_STRIDE = 1792;
constexpr int WS_RED = 1536 << 10;
constexpr int WS_O = 2 << 20;
constexpr int WS_U = 5 << 20;
constexpr int U_STRIDE = 4096 + 128;
constexpr int WS_USED = 8 << 20;

struct Params {
    const char* x; const char* wqkv; const char* wphi; const char* wo;
    const char* q; const char* k; const char* v; const char* shk; const char* hk; const char* dv;
    char* out; char* ws;
};

template <int V> struct ic { static constexpr int value = V; };
template <int B, int E, class Fn> __device__ __forceinline__ void static_for(Fn&& fn) {
    if constexpr (B < E) { fn(ic<B>{}); static_for<B + 1, E>(fn); }
}
#define END_WAVE() asm volatile("s_endpgm" ::: "memory")
#define SBAR() __builtin_amdgcn_sched_barrier(0)
#define FENCE() asm volatile("" ::: "memory")

__device__ __forceinline__ void dma16(const char* sbase, unsigned voff, unsigned lds_dst) {
    asm volatile("s_mov_b32 m0, %2\n\ts_nop 0\n\tglobal_load_lds_dwordx4 %0, %1" :: "v"(voff), "s"(sbase), "s"(lds_dst) : "memory");
}
__device__ __forceinline__ void set_m0(unsigned d) { asm volatile("s_mov_b32 m0, %0\n\ts_nop 0" :: "s"(d) : "memory"); }
__device__ __forceinline__ void dma_off(int off, const char* sbase, unsigned voff) {
    asm volatile("global_load_lds_dwordx4 %0, %1 offset:%2" :: "v"(voff), "s"(sbase), "i"(off) : "memory");
}
__device__ __forceinline__ void dma_once(int off, const char* sbase, unsigned voff) {
    asm volatile("global_load_lds_dwordx4 %0, %1 offset:%2 nt" :: "v"(voff), "s"(sbase), "i"(off) : "memory");
}
__device__ __forceinline__ void bar_raw() { asm volatile("s_barrier" ::: "memory"); }
__device__ __forceinline__ lds_ptr opaque(lds_ptr p) { asm volatile("" : "+v"(p)); return p; }
template <int N> __device__ __forceinline__ void wait_vm() { asm volatile("s_waitcnt vmcnt(%0)" :: "i"(N) : "memory"); }
__device__ __forceinline__ void bar() { asm volatile("s_waitcnt lgkmcnt(0)\n\ts_barrier" ::: "memory"); }
__device__ __forceinline__ void touch(const char* sbase, unsigned voff, unsigned lds_dst) {
    asm volatile("s_mov_b32 m0, %2\n\ts_nop 0\n\tglobal_load_lds_dword %0, %1" :: "v"(voff), "s"(sbase), "s"(lds_dst) : "memory");
}
__device__ __forceinline__ u32x4 lds_r128(lds_ptr p) { return *(__attribute__((address_space(3))) u32x4*)p; }
__device__ __forceinline__ u32x2 lds_r64(lds_ptr p) { return *(__attribute__((address_space(3))) u32x2*)p; }
__device__ __forceinline__ unsigned lds_r32(lds_ptr p) { return *(__attribute__((address_space(3))) unsigned*)p; }
__device__ __forceinline__ void lds_w128(lds_ptr p, u32x4 v) { *(__attribute__((address_space(3))) u32x4*)p = v; }
__device__ __forceinline__ void lds_w64(lds_ptr p, u32x2 v) { *(__attribute__((address_space(3))) u32x2*)p = v; }
__device__ __forceinline__ void lds_w32(lds_ptr p, unsigned v) { *(__attribute__((address_space(3))) unsigned*)p = v; }
__device__ __forceinline__ u32x2 lds_tr(lds_ptr p) {
    return __builtin_bit_cast(u32x2, __builtin_amdgcn_ds_read_tr16_b64_v4i16((__attribute__((address_space(3))) s16x4*)p));
}
__device__ __forceinline__ u32x4 cat(u32x2 a, u32x2 b) { return u32x4{a.x, a.y, b.x, b.y}; }
__device__ __forceinline__ f32x4 mfma32(u32x4 a, u32x4 b, f32x4 c) {
    return __builtin_amdgcn_mfma_f32_16x16x32_bf16(__builtin_bit_cast(bf16x8, a), __builtin_bit_cast(bf16x8, b), c, 0, 0, 0);
}
__device__ __forceinline__ f32x16 mfma_w(u32x4 a, u32x4 b, f32x16 c) {
    return __builtin_amdgcn_mfma_f32_32x32x16_bf16(__builtin_bit_cast(bf16x8, a), __builtin_bit_cast(bf16x8, b), c, 0, 0, 0);
}
__device__ __forceinline__ unsigned pk(float lo, float hi) {
    return __builtin_bit_cast(unsigned, __builtin_convertvector(f32x2{lo, hi}, bf16x2));
}
__device__ __forceinline__ float blo(unsigned x) { return __uint_as_float(x << 16); }
__device__ __forceinline__ float bhi(unsigned x) { return __uint_as_float(x & 0xffff0000u); }

__device__ __forceinline__ u64 ld_agent(const char* p) { return __hip_atomic_load((const u64*)p, __ATOMIC_RELAXED, __HIP_MEMORY_SCOPE_AGENT); }
__device__ __forceinline__ void st_agent(char* p, u64 v) { __hip_atomic_store((u64*)p, v, __ATOMIC_RELAXED, __HIP_MEMORY_SCOPE_AGENT); }

__device__ __forceinline__ void wait_flags(const char* flags, int n, int lane, u64 epoch) {
    const char* p = flags + lane * 128;
    bool ok = lane >= n;
    for (int spin = 0; spin < (1 << 17); ++spin) {
        if (!ok) ok = ld_agent(p) == epoch;
        if (__all(ok)) break;
        __builtin_amdgcn_s_sleep(2);
    }
    FENCE();
}
__device__ __forceinline__ void grp_post(lds_ptr c, int lane) {
    FENCE();
    if (lane == 0) __hip_atomic_fetch_add((__attribute__((address_space(3))) unsigned*)c, 1u, __ATOMIC_RELAXED, __HIP_MEMORY_SCOPE_WORKGROUP);
}
template <int SLEEP> __device__ __forceinline__ void grp_wait(lds_ptr c, unsigned target) {
    for (int spin = 0; spin < (1 << 20); ++spin) {
        FENCE();
        if ((unsigned)__builtin_amdgcn_readfirstlane(lds_r32(c)) >= target) break;
        if (SLEEP) __builtin_amdgcn_s_sleep(SLEEP);
    }
    FENCE();
}

constexpr int K_THR = 1, K_THR0 = 0, K_THR1 = 8, KV_PAUSE_N = 18, KV_PAUSE_W = 3;

constexpr unsigned EMPTY = 0xffffffffu;
constexpr int SPIN_MAX = 1 << 17;
__device__ __forceinline__ unsigned pkn(float lo, float hi) { return __builtin_elementwise_min(pk(lo, hi), EMPTY - 1u); }
__device__ __forceinline__ unsigned umax4(u32x4 v) { return __builtin_elementwise_max(__builtin_elementwise_max(v.x, v.y), __builtin_elementwise_max(v.z, v.w)); }
__device__ __forceinline__ u32x4 ld_ws(__amdgpu_buffer_rsrc_t r, int voff, int soff) { return __builtin_amdgcn_raw_buffer_load_b128(r, voff, soff, 16); }
__device__ __forceinline__ void st_ws(__amdgpu_buffer_rsrc_t r, int voff, u32x4 v) { __builtin_amdgcn_raw_buffer_store_b128(v, r, voff, 0, 16); }
__device__ __forceinline__ bool missing(u32x4 a, u32x4 b) { return __any(__builtin_elementwise_max(umax4(a), umax4(b)) == EMPTY); }
__device__ __forceinline__ void fetch2(const char* sbase, unsigned v0, unsigned v1, u32x4& a, u32x4& b) {
    for (int spin = 0; spin < SPIN_MAX; ++spin) {
        asm volatile("global_load_dwordx4 %0, %2, %4 sc1\n\tglobal_load_dwordx4 %1, %3, %4 sc1\n\ts_waitcnt vmcnt(0)"
                     : "=&v"(a), "=&v"(b) : "v"(v0), "v"(v1), "s"(sbase) : "memory");
        if (!missing(a, b)) break;
        __builtin_amdgcn_s_sleep(3);
    }
}

#define MX3(a, b, c) __builtin_fmaxf(__builtin_fmaxf((a), (b)), (c))
#define MN3(a, b, c) __builtin_fminf(__builtin_fminf((a), (b)), (c))

constexpr float L2E = 1.4426950408889634f;
struct Range {
    float x[8], n[8], cp, cn;
    __device__ __forceinline__ void step(int i, const f32x16& a, const f32x16& b, int o) {
        if (i == 0) { x[0] = MX3(b[o], b[o + 1], b[o + 2]); n[0] = MN3(b[o], b[o + 1], b[o + 2]); x[1] = MX3(b[o + 3], a[0], a[1]); n[1] = MN3(b[o + 3], a[0], a[1]); }
        if (i == 1) { x[2] = MX3(a[2], a[3], a[4]); n[2] = MN3(a[2], a[3], a[4]); x[3] = MX3(a[5], a[6], a[7]); n[3] = MN3(a[5], a[6], a[7]); }
        if (i == 2) { x[4] = MX3(x[0], a[8], a[9]); n[4] = MN3(n[0], a[8], a[9]); x[5] = MX3(x[1], a[10], a[11]); n[5] = MN3(n[1], a[10], a[11]); }
        if (i == 3) { x[6] = MX3(x[2], a[12], a[13]); n[6] = MN3(n[2], a[12], a[13]); x[7] = MX3(x[3], a[14], a[15]); n[7] = MN3(n[3], a[14], a[15]); }
        if (i == 4) { cp = __builtin_fmaxf(MX3(x[4], x[5], x[6]), x[7]) * -L2E; cn = __builtin_fminf(MN3(n[4], n[5], n[6]), n[7]) * L2E; }
    }
    __device__ __forceinline__ void all(const f32x16& a, const f32x16& b, int o) {
#pragma unroll
        for (int i = 0; i < 5; ++i) step(i, a, b, o);
    }
};
template <bool SCALED, class Fill, class Emit>
__device__ __forceinline__ float dual_softmax(const f32x16& pa, const f32x16& pb, int o, const Range& rg, unsigned (&F)[20], Fill&& fill, Emit&& emit) {
    float p[20];
#pragma unroll
    for (int e = 0; e < 20; ++e) p[e] = e < 16 ? pa[e] : pb[o + e - 16];
    const float cp = rg.cp, cn = rg.cn;
    f32x2 ep[10], en[10];
    SBAR(); fill(0); SBAR();
#pragma unroll
    for (int i = 0; i < 10; ++i) {
        const f32x2 v = {p[2 * i], p[2 * i + 1]};
        ep[i] = __builtin_elementwise_fma(v, f32x2{L2E, L2E}, f32x2{cp, cp});
        en[i] = __builtin_elementwise_fma(v, f32x2{-L2E, -L2E}, f32x2{cn, cn});
    }
    auto ex = [&](f32x2& v) { v = f32x2{__builtin_amdgcn_exp2f(v.x), __builtin_amdgcn_exp2f(v.y)}; };
    SBAR(); fill(1); SBAR();
    ex(ep[0]); ex(ep[1]);
    SBAR(); fill(2); SBAR();
    ex(ep[2]); ex(ep[3]);
    SBAR(); fill(3); SBAR();
    ex(ep[4]); ex(ep[5]);
    SBAR(); fill(4); SBAR();
    ex(ep[6]); ex(ep[7]);
    SBAR(); fill(5); SBAR();
    ex(ep[8]); ex(ep[9]);
    SBAR();
    const f32x2 a0 = ep[0] + ep[1], a1 = ep[2] + ep[3], a2 = ep[4] + ep[5], a3 = ep[6] + ep[7];
    SBAR();
    const f32x2 a4 = ep[8] + ep[9], a5 = a0 + a1, a6 = a2 + a3;
    ex(en[0]);
    SBAR();
    const f32x2 a7 = a5 + a4;
    ex(en[1]);
    SBAR();
    const f32x2 sp = a7 + a6;
    ex(en[2]);
    SBAR();
    const float s1 = sp.x + sp.y;
    ex(en[3]);
    SBAR();
    if (!SCALED) {
        const float rp = __builtin_amdgcn_rcpf(s1);
        SBAR();
        ex(en[4]);
        SBAR();
#pragma unroll
        for (int i = 0; i < 10; ++i) ep[i] *= rp;
    } else ex(en[4]);
    SBAR();
#pragma unroll
    for (int i = 0; i < 4; ++i) F[i] = pk(ep[i].x, ep[i].y);
    SBAR(); emit(0); SBAR();
#pragma unroll
    for (int i = 4; i < 8; ++i) F[i] = pk(ep[i].x, ep[i].y);
    ex(en[5]); ex(en[6]);
    SBAR(); emit(1); SBAR();
    ex(en[7]); ex(en[8]); ex(en[9]);
    SBAR();
    const f32x2 b0 = en[0] + en[1], b1 = en[2] + en[3], b2 = en[4] + en[5];
    SBAR();
    const f32x2 sn = ((b0 + b1) + (en[6] + en[7])) + (b2 + (en[8] + en[9]));
    float rn = __builtin_amdgcn_rcpf(sn.x + sn.y);
    SBAR();
    F[8] = pk(ep[8].x, ep[8].y); F[9] = pk(ep[9].x, ep[9].y);
    SBAR();
    if (SCALED) rn *= s1;
#pragma unroll
    for (int i = 0; i < 6; ++i) en[i] *= rn;
    SBAR();
    F[10] = pk(en[0].x, en[0].y); F[11] = pk(en[1].x, en[1].y);
    SBAR(); emit(2); SBAR();
#pragma unroll
    for (int i = 6; i < 10; ++i) en[i] *= rn;
#pragma unroll
    for (int i = 2; i < 6; ++i) F[10 + i] = pk(en[i].x, en[i].y);
    SBAR(); emit(3); SBAR();
#pragma unroll
    for (int i = 6; i < 10; ++i) F[10 + i] = pk(en[i].x, en[i].y);
    SBAR(); emit(4); SBAR();
    return s1;
}

struct Projection {
    u32x4 xf[5];
    f32x16 a[4];
    __device__ __forceinline__ void product(int i, const u32x4 (&W)[11]) {
        constexpr int dst[11] = {2, 0, 2, 0, 2, 0, 3, 1, 3, 1, 1}, src[11] = {-1, -1, 2, 0, 2, 0, 2, -1, 3, 1, 1};
        constexpr int wi[11] = {6, 0, 7, 1, 10, 2, 8, 3, 9, 4, 5}, xi[11] = {0, 0, 1, 1, 4, 4, 2, 2, 3, 3, 4};
        a[dst[i]] = mfma_w(W[wi[i]], xf[xi[i]], src[i] < 0 ? f32x16{} : a[src[i] < 0 ? 0 : src[i]]);
    }
};

constexpr int A_WPHI = 157696, A_ZERO = A_WPHI + 3200, A_EPOCH = A_ZERO + 2304;
constexpr int C_KV = A_ZERO + 2272, C_SLOT = C_KV + 4, C_DEAD = C_KV + 8, C_O = C_KV + 12, C_WO = C_KV + 16, C_BUMP = C_KV + 20, C_END = C_KV + 24;
constexpr int A_WQ = 65536;
constexpr int wq_pos(int u) { return A_WQ + (u < 16 ? u + 2 : u - 16) * 5120; }
constexpr int wo_pos(int p) { return p < 4 ? p * 10240 : wq_pos(2) + (p - 4) * 10240; }
constexpr int A_OST = wq_pos(10);
constexpr int W_PRE = 16, O_AHEAD = 9, O_ASK = 3, O_CHK = 11, TAIL = 4;

__device__ __forceinline__ unsigned wo_voff(int lane) { return (lane >> 3) * 1280 + (((lane & 7) ^ ((lane >> 2) & 4)) * 16) + 4096; }
__device__ __forceinline__ void stage_wo(const Params& P, unsigned lds0, int ct, int rot, int p, unsigned voff) {
    const char* s = P.wo + ct * 128 + ((p + rot) & 7) * (80 * 1280);
    const unsigned d = lds0 + p * 10240 + (p >= 4 ? wo_pos(4) - 40960 : 0);
#pragma unroll
    for (int h = 0; h < 2; ++h) {
        set_m0(d + h * 5120 + 4096);
#pragma unroll
        for (int j = 0; j < 5; ++j) dma_off((j - 4) * 1024, s + h * 51200 + j * (10240 - 1024), voff);
    }
}

__device__ __forceinline__ void out_tile(const Params& P, lds_ptr L, int r, int ct, int rot, int wv, int lane) {
    const __amdgpu_buffer_rsrc_t wsr = __builtin_amdgcn_make_buffer_rsrc((void*)P.ws, 0, WS_USED, 0x00020000);
    const int hi = lane >> 5, g1 = (lane >> 4) & 1, tq = (lane & 15) >> 2, tp = lane & 3;
    lds_ptr wb[2][2];
    const int wo_ = (hi * 20 + tq) * 128 + g1 * 32 + (tp & 1) * 16 + (tp >> 1) * 8;
#pragma unroll
    for (int h = 0; h < 2; ++h)
#pragma unroll
        for (int t = 0; t < 2; ++t) wb[h][t] = opaque(L + wo_pos(4 * h) + wo_ + ((t ^ (tq >> 1)) * 64));
    u32x2 wh[160];
    u32x4 of[40];
    auto step_of = [](int m) { return m < 80 - 2 * TAIL ? m >> 1 : 40 - TAIL + (m - (80 - 2 * TAIL)) % TAIL; };
    auto tile_of = [](int m) { return m < 80 - 2 * TAIL ? m & 1 : (m - (80 - 2 * TAIL)) / TAIL; };
    auto rd = [&](int n) {
        const int s = step_of(n >> 1), t = tile_of(n >> 1), up = n & 1, u = s / 20, e = s % 5, p = 2 * (s % 20 / 5) + (e < 4 ? e >> 1 : up);
        wh[n] = lds_tr(wb[p >> 2][t] + (p & 3) * 10240 + (u * 40 + (e < 4 ? (e & 1) * 8 + 4 * up : 16)) * 128);
        SBAR();
    };
    const int obase = WS_O + (r * 4 + wv) * 8 * 5120;
    auto ld = [&](int s) {
        const int u = s / 20, e = s % 5, p = 2 * (s % 20 / 5);
        if (e < 4) of[s] = ld_ws(wsr, lane * 16 + u * 2560 + (e & 1) * 1024, obase + ((p + (e >> 1) + rot) & 7) * 5120);
        else of[s] = cat(__builtin_amdgcn_raw_buffer_load_b64(wsr, lane * 8 + 2048, obase + ((p + rot) & 7) * 5120 + u * 2560, 16),
                         __builtin_amdgcn_raw_buffer_load_b64(wsr, lane * 8 + 2048, obase + ((p + 1 + rot) & 7) * 5120 + u * 2560, 16));
    };
    auto last_word = [&](int u) { return __builtin_amdgcn_raw_buffer_load_b32(wsr, (lane & 7) * 5120 + 2044, obase + u * 2560, 16); };
    auto arrive = [&](int u) {
        for (int spin = 0; spin < SPIN_MAX; ++spin) {
            FENCE();
            if (!__any(last_word(u) == EMPTY)) break;
            __builtin_amdgcn_s_sleep(1);
        }
        FENCE();
    };
    f32x16 acc[2];
    lds_ptr st = L + A_OST + wv * 4096;
    auto put = [&](int c) {
        const f32x16& a = acc[c >> 1];
        const int e = (c & 1) * 8, tok = lane & 31;
        lds_w128(st + tok * 128 + (((2 * c + hi) ^ (tok & 7)) * 16),
                 u32x4{pk(a[e], a[e + 1]), pk(a[e + 2], a[e + 3]), pk(a[e + 4], a[e + 5]), pk(a[e + 6], a[e + 7])});
    };
#pragma nounroll
    for (int pass = 0; pass < 2; ++pass) {
        FENCE();
#pragma unroll
        for (int n = 0; n < W_PRE; ++n) rd(n);
#pragma unroll
        for (int n = 0; n < W_PRE; n += 2) asm volatile("" : "+v"(wh[n]), "+v"(wh[n + 1]));
        __builtin_amdgcn_s_setprio(2);
        if (blockIdx.x < 128) { grp_wait<3>(L + C_O, 4); __builtin_amdgcn_s_sleep(10); }
        arrive(0);
        if (blockIdx.x < 128) grp_wait<1>(L + C_END, 4);
#pragma unroll
        for (int s = 0; s < O_AHEAD; ++s) ld(s);
        if (blockIdx.x == 0) grp_post(L + C_BUMP, lane);
        acc[0] = f32x16{}; acc[1] = f32x16{};
        unsigned second;
        SBAR();
        static_for<0, 80>([&](auto M) {
            constexpr int m = M.value, s = m >> 1;
            constexpr int n0 = W_PRE + (m * (160 - W_PRE) + 71) / 72, n1 = W_PRE + ((m + 1) * (160 - W_PRE) + 71) / 72;
            acc[tile_of(m)] = mfma_w(cat(wh[2 * m], wh[2 * m + 1]), of[step_of(m)], acc[tile_of(m)]);
            SBAR();
#pragma unroll
            for (int n = n0; n < n1; ++n) if (n < 160) rd(n);
            if constexpr (m & 1) {
                if constexpr (s + O_AHEAD < 20 || (s > O_CHK && s + O_AHEAD < 40)) ld(s + O_AHEAD);
                if constexpr (s == O_ASK) second = last_word(1);
                if constexpr (s == O_CHK) {
                    if (__any(second == EMPTY)) arrive(1);
                    FENCE();
#pragma unroll
                    for (int e = 20; e <= s + O_AHEAD; ++e) ld(e);
                }
                SBAR();
            }
            if constexpr (m >= 78) { put(m - 78); SBAR(); }
        });
        if (pass || !__any(acc[0][0] != acc[0][0])) break;
#pragma nounroll
        for (int e = 0; e < 20; ++e) { u32x4 a, b; fetch2(P.ws + obase + e * 2048, lane * 16, lane * 16 + 1024, a, b); }
    }
    put(2); put(3);
    FENCE();
    const __amdgpu_buffer_rsrc_t outr = __builtin_amdgcn_make_buffer_rsrc((void*)P.out, 0, 2048 * 2560, 0x00020000);
    const int ob = (r * 128 + wv * 32 + (lane >> 3)) * 2560 + ct * 128 + (lane & 7) * 16;
#pragma unroll
    for (int j = 0; j < 4; ++j)
        __builtin_amdgcn_raw_buffer_store_b128(lds_r128(st + j * 1024 + (lane >> 3) * 128 + (((lane & 7) ^ (lane >> 3)) * 16)), outr, ob + j * 8 * 2560, 0, 2);
}

constexpr int A_WV = 10368, A_X = 20608, A_SLOT = 36992;
constexpr int A_PHI = 0, A_V = 40960;

struct WphiBases { lds_ptr b0, b1, b2, r0, r1, r2; };
__device__ __forceinline__ WphiBases wphi_bases(lds_ptr L, int lane, int c0, int c4) {
    const int hi = lane >> 5, g1 = (lane >> 4) & 1, tq = (lane & 15) >> 2, tp = lane & 3;
    const bool act = hi == (tp & 1);
    const int gam = 2 * g1 + (tp >> 1), o = A_WPHI + hi * 800 + tq * 40;
    WphiBases w;
    w.b0 = opaque(L + (act ? o + gam * 8 + c0 * 160 : A_ZERO));
    w.r0 = opaque(L + (act ? o + gam * 8 + c4 * 160 : A_ZERO));
    w.b1 = opaque(L + (act && gam == 0 ? o + 32 + c0 * 160 : A_ZERO));
    w.r1 = opaque(L + (act && gam == 0 ? o + 32 + c4 * 160 : A_ZERO));
    w.b2 = opaque(L + (act && gam == 1 ? o + 1632 + c0 * 160 : A_ZERO));
    w.r2 = opaque(L + (act && gam == 1 ? o + 1632 + c4 * 160 : A_ZERO));
    return w;
}
struct WphiOps {
    u32x2 h[20];
    __device__ __forceinline__ void read(int e, const WphiBases& w) {
        if (e < 10) h[e] = e % 5 < 4 ? lds_tr(w.b0 + (e / 5) * 1600 + (e % 5) * 160) : lds_tr(w.r0 + (e / 5) * 1600);
        else h[e] = e % 5 < 4 ? lds_tr((e < 15 ? w.b1 : w.b2) + (e % 5) * 160) : lds_tr(e < 15 ? w.r1 : w.r2);
    }
    __device__ __forceinline__ void form(u32x4 (&wp)[11]) const {
        const u32x2 z2 = {0u, 0u};
        wp[0] = cat(h[0], h[1]); wp[1] = cat(h[2], h[3]); wp[2] = cat(h[4], z2);
        wp[3] = cat(h[5], h[6]); wp[4] = cat(h[7], h[8]); wp[5] = cat(z2, h[9]);
        wp[6] = cat(h[10], h[11]); wp[7] = cat(h[12], h[13]); wp[8] = cat(h[15], h[16]); wp[9] = cat(h[17], h[18]);
        wp[10] = cat(h[14], h[19]);
    }
};

__device__ __forceinline__ void role_k(const Params& P, lds_ptr L, int r, int g, int ct, int rot, int wv) {
    const int tid = threadIdx.x, lane = tid & 63;
    const unsigned lds0 = (unsigned)(uintptr_t)L;
    const int n32 = lane & 31, hi = lane >> 5, g1 = (lane >> 4) & 1, tq = (lane & 15) >> 2, tp = lane & 3;
    const __amdgpu_buffer_rsrc_t wsr = __builtin_amdgcn_make_buffer_rsrc((void*)P.ws, 0, WS_USED, 0x00020000);
    const int set = (g * 4 + wv) * 16, pbase = WS_PART + (set + r) * PART_STRIDE, rbase = WS_RED + (g * 4 + wv) * PART_STRIDE;
    __builtin_amdgcn_s_setprio(2);
    if (tid < 144) lds_w128(L + A_ZERO + tid * 16, u32x4{0, 0, 0, 0});

    lds_ptr xa[2][4], wa[2];
#pragma unroll
    for (int h = 0; h < 2; ++h) {
#pragma unroll
        for (int ks = 0; ks < 4; ++ks) xa[h][ks] = opaque(L + h * 2 * A_SLOT + (wv * 32 + n32) * 128 + (((2 * ks + hi) ^ ((n32 >> 1) & 7)) * 16));
        wa[h] = opaque(L + h * 2 * A_SLOT + g1 * A_WV + (8 * hi + tq) * 160 + tp * 40);
    }
    f32x16 acc[5] = {};
    u32x4 xf[3], wp[11];
    u32x2 wlo[3][5], whi[3][5];
    auto rd_main = [&](int s, int e) {
        const int kb = s >> 2, ks = s & 3, h = (kb >> 1) & 1, o = (kb & 1) * A_SLOT;
        if (e == 10) xf[s % 3] = lds_r128(xa[h][ks] + o + A_X);
        else if (e & 1) whi[s % 3][e >> 1] = lds_tr(wa[h] + o + ks * 2560 + 640 + (e >> 1) * 8);
        else wlo[s % 3][e >> 1] = lds_tr(wa[h] + o + ks * 2560 + (e >> 1) * 8);
        SBAR();
    };
    const WphiBases wb = wphi_bases(L, lane, 0, 4);
    WphiOps wo;
    const int tok = lane & 15, gq = lane >> 4, token = wv * 32 + n32, trow = 16 * hi + 8 * g1 + 2 * tq;
    lds_ptr vp = opaque(L + A_V + hi * 6144 + token * 48), pp = opaque(L + A_PHI + hi * 10240 + token * 80);
    lds_ptr pa = opaque(L + A_PHI + wv * 10240 + trow * 80 + tp * 8), pb = opaque(L + A_V + wv * 6144 + trow * 48 + tp * 8);
    int o4a = pbase + lane * 16;
    int o2a = hi == 0 ? pbase + 1024 + lane * 8 : WS_USED;
    int o4b = tok < 5 ? pbase + 1280 + (gq * 5 + tok) * 16 : WS_USED;
    int o2b = (tok < 5 && hi == 0) ? pbase + 1600 + (gq * 5 + tok) * 8 : WS_USED;
    unsigned sv = (lane >> 3) * PART_STRIDE + (r < 13 ? r * 128 + (lane & 7) * 16 : 13 * 128), sw = sv + 8 * PART_STRIDE;
    int rv = (lane & 40) == 0 && r < 14 ? rbase + r * 128 + (lane & 7) * 16 + g1 * 8 : WS_USED;
    unsigned r0 = lane * 16, r1 = 1024 + (lane < 41 ? lane : 40) * 16;
    unsigned wov = wo_voff(lane);
    u32x4 sel;
#pragma unroll
    for (int e = 0; e < 4; ++e) sel[e] = (lane & 14) == 2 * e ? ((lane & 1) ? 0x3f800000u : 0x00003f80u) : 0u;
    lds_ptr kw0, kw1, kw2;
    {
        int b0, b1;
        if (lane < 16) { b0 = ((2 * lane) & 15) * 80 + 64 + (lane >> 3) * 8; b1 = b0 + 80; }
        else if (lane < 36) { const int e = lane - 16, gg = e / 5; b0 = (16 + e - gg * 5) * 80 + gg * 8; b1 = b0 + 32; }
        else if (lane < 41) { const int e = 2 * (lane - 36), g0 = e / 5, g2 = (e + 1) / 5; b0 = (16 + e - g0 * 5) * 80 + 64 + g0 * 8; b1 = (17 + e - g2 * 5) * 80 + 64 + g2 * 8; }
        else { b0 = 2048 + lane * 16; b1 = b0 + 8; }
        lds_ptr kb = L + A_V + wv * 6144;
        kw0 = opaque(kb + tok * 80 + gq * 8); kw1 = opaque(kb + b0); kw2 = opaque(kb + b1);
    }
    asm volatile("" : "+v"(o4a), "+v"(o2a), "+v"(o4b), "+v"(o2b), "+v"(sv), "+v"(sw), "+v"(rv), "+v"(r0), "+v"(r1), "+v"(wov), "+v"(sel));
    wait_vm<0>();
    bar();
#pragma unroll
    for (int e = 0; e < 22; ++e) rd_main(e / 11, (e + 10) % 11);
    static_for<0, 200>([&](auto G) {
        constexpr int gap = G.value, s = gap / 5, t = gap % 5, kb = s >> 2;
        if constexpr (t == 0) {
            if constexpr ((s & 3) == 2 && kb < 9) bar_raw();
            if constexpr (K_THR > 0 && (s & 3) == 0 && kb >= K_THR0 && kb < K_THR1) __builtin_amdgcn_s_sleep(K_THR);
            if constexpr (s == 39) bar(); else if constexpr (s > 0) __builtin_amdgcn_s_waitcnt(0xCB7F);
            SBAR();
        }
        acc[t] = mfma_w(cat(wlo[s % 3][t], whi[s % 3][t]), xf[s % 3], acc[t]);
        SBAR();
        if constexpr (s < 38) { rd_main(s + 2, 2 * t); rd_main(s + 2, 2 * t + 1); if constexpr (t == 2) rd_main(s + 2, 10); }
        else { wo.read((gap - 190) * 2, wb); wo.read((gap - 190) * 2 + 1, wb); SBAR(); }
    });
    wo.form(wp);
    {
        auto pair = [&](int t, int c) { return u32x2{pk(acc[t][4 * c], acc[t][4 * c + 1]), pk(acc[t][4 * c + 2], acc[t][4 * c + 3])}; };
        auto put_v = [&](int u, int c) { lds_w128(vp + u * 12288 + c * 16, cat(pair(2 * c, 2 + u), c < 2 ? pair(2 * c + 1, 2 + u) : u32x2{0x00003f80u, 0u})); };
        Projection pr;
        pr.xf[0] = cat(pair(0, 0), pair(1, 0));
#pragma unroll
        for (int i = 0; i < 6; ++i) {
            SBAR();
            pr.product(i, wp);
            SBAR();
            if (i == 0) pr.xf[1] = cat(pair(2, 0), pair(3, 0));
            if (i == 1) { pr.xf[4] = cat(pair(4, 0), pair(4, 1)); pr.xf[2] = cat(pair(0, 1), pair(1, 1)); }
            if (i >= 2) put_v((i - 2) / 3, (i - 2) % 3);
            if (i == 5) { put_v(1, 1); pr.xf[3] = cat(pair(2, 1), pair(3, 1)); }
        }
        SBAR();
        unsigned F[2][20];
        auto wr = [&](int u, int c) { lds_w128(pp + u * 20480 + c * 16, u32x4{F[u][4 * c], F[u][4 * c + 1], F[u][4 * c + 2], F[u][4 * c + 3]}); };
        Range rg;
        rg.all(pr.a[0], pr.a[2], 0);
        dual_softmax<false>(pr.a[0], pr.a[2], 0, rg, F[0], [&](int i) { if (i) pr.product(5 + i, wp); else put_v(1, 2); }, [&](int c) { if (c < 2) wr(0, c); });
        rg.all(pr.a[1], pr.a[3], 4);
        dual_softmax<false>(pr.a[1], pr.a[3], 4, rg, F[1], [&](int i) { if (!(i & 1)) wr(0, 2 + (i >> 1)); }, [&](int c) { wr(1, c); });
    }
    bar();
    {
        u32x4 vb[4][2], pf[4][3];
        auto rd = [&](int ks, int e) {
            if (e == 0) vb[ks][0] = cat(lds_tr(pb + ks * 1536), lds_tr(pb + ks * 1536 + 48));
            else if (e == 2) vb[ks][1] = cat(lds_tr(pb + ks * 1536 + 32), lds_tr(pb + ks * 1536 + 80));
            else { const int f = e == 1 ? 0 : e - 2; pf[ks][f] = cat(lds_tr(pa + ks * 2560 + f * 32), lds_tr(pa + ks * 2560 + 80 + f * 32)); }
        };
#pragma unroll
        for (int e = 0; e < 7; ++e) { rd(e / 5, e % 5); SBAR(); }
        f32x4 kv[2][3] = {};
#pragma unroll
        for (int m = 0; m < 24; ++m) {
            const int ks = m / 6, f = (m % 6) >> 1, d = m & 1;
            if (m + 7 < 20) rd((m + 7) / 5, (m + 7) % 5);
            kv[d][f] = mfma32(pf[ks][f], vb[ks][d], kv[d][f]);
            SBAR();
        }
#pragma unroll
        for (int d = 0; d < 2; ++d)
            st_ws(wsr, d ? o4b : o4a, u32x4{pkn(kv[d][0][0], kv[d][0][1]), pkn(kv[d][0][2], kv[d][0][3]), pkn(kv[d][1][0], kv[d][1][1]), pkn(kv[d][1][2], kv[d][1][3])});
#pragma unroll
        for (int d = 0; d < 2; ++d)
            __builtin_amdgcn_raw_buffer_store_b64(u32x2{pkn(kv[d][2][0], kv[d][2][1]), pkn(kv[d][2][2], kv[d][2][3])}, wsr, d ? o2b : o2a, 0, 16);
    }
    const u32x2 ev = lds_r64(L + A_EPOCH);
    const u64 epoch = ((u64)ev.y << 32) | ev.x;
    u32x4 fa, fb;
    stage_wo(P, lds0, ct, rot, wv, wov);
    __builtin_amdgcn_s_sleep(3);
    fetch2(P.ws + WS_PART + set * PART_STRIDE, sv, sw, fa, fb);
    {
        f32x4 d = mfma32(sel, fb, mfma32(sel, fa, f32x4{0.f, 0.f, 0.f, 0.f}));
#pragma unroll
        for (int e = 0; e < 4; ++e) { float t = d[e]; t += __int_as_float(__builtin_amdgcn_update_dpp(0, __float_as_int(t), 0x128, 0xf, 0xf, true)); asm volatile("" : "+v"(t)); d[e] = t; }
        __builtin_amdgcn_raw_buffer_store_b64(u32x2{pkn(d[0], d[1]), pkn(d[2], d[3])}, wsr, rv, 0, 16);
    }
    const u64 oset = ld_agent(P.ws + WS_FLAGO + r * 128);
    grp_wait<2>(L + C_DEAD, 4);
    stage_wo(P, lds0, ct, rot, 4 + wv, wov);
    __builtin_amdgcn_s_sleep(2);
    fetch2(P.ws + rbase, r0, r1, fa, fb);
    lds_w64(kw0, u32x2{fa.x, fa.y}); lds_w64(kw0 + 32, u32x2{fa.z, fa.w});
    lds_w64(kw1, u32x2{fb.x, fb.y}); lds_w64(kw2, u32x2{fb.z, fb.w});
    if (!__all(oset == epoch)) wait_flags(P.ws + WS_FLAGO + r * 128, 1, lane, epoch);
    grp_post(L + C_KV, lane);
    if ((g & 3) == 0 && tid == 0) st_agent(P.ws + WS_FLAGU + (r * 2 + (g >> 2)) * 128, epoch);
    __builtin_amdgcn_s_setprio(0);
    wait_vm<0>();
    grp_post(L + C_WO, lane);
    grp_wait<2>(L + C_WO, 4);
}

__device__ __forceinline__ void role_q(const Params& P, lds_ptr L, int r, int g, int wv) {
    const int lane = threadIdx.x & 63;
    const unsigned lds0 = (unsigned)(uintptr_t)L;
    const int n32 = lane & 31, hi = lane >> 5, g1 = (lane >> 4) & 1, tq = (lane & 15) >> 2, tp = lane & 3;
    unsigned xo[2], wo5[5];
#pragma unroll
    for (int j = 0; j < 5; ++j) wo5[j] = (((j * 64 + lane) * 6554) >> 16) * (3840 - 160) + lane * 16 + 1024;
    const char* xsb = P.x + (r * 128 + wv * 32) * 1280;
    const char* wsb = P.wqkv + 1280 * (1 + (wv & 1)) + g * 160 + (wv >> 1) * (32 * 3840);
    auto wq_src = [&](int u0) { return P.wqkv + g * 160 + (u0 + wv) * (32 * 3840); };
    auto stage_w = [&](const char* s, unsigned d) {
        set_m0(d + 1024);
#pragma unroll
        for (int j = 0; j < 5; ++j) dma_off((j - 1) * 1024, s, wo5[j]);
    };
    auto stage = [&](int n) {
        stage_w(wsb + n * (64 * 3840), lds0 + (wv & 1) * A_WV + (wv >> 1) * 5120 + (n & 3) * A_SLOT);
        set_m0(lds0 + A_X + wv * 4096 + (n & 3) * A_SLOT);
#pragma unroll
        for (int j = 0; j < 4; ++j) dma_off(j * 1024, xsb + n * 128 + j * (8 * 1280 - 1024), xo[j & 1]);
    };
#pragma unroll
    for (int j = 0; j < 2; ++j) xo[j] = (lane >> 3) * 1280 + (((lane & 7) ^ (4 * j + (lane >> 4))) * 16);
    const __amdgpu_buffer_rsrc_t wsr = __builtin_amdgcn_make_buffer_rsrc((void*)P.ws, 0, WS_USED, 0x00020000);
    stage(1);
    __builtin_amdgcn_s_setprio(3);
    u32x4 xk[40];
    {
        lds_ptr xa[4];
#pragma unroll
        for (int ks = 0; ks < 4; ++ks) xa[ks] = opaque(L + A_X + (wv * 32 + n32) * 128 + (((2 * ks + hi) ^ ((n32 >> 1) & 7)) * 16));
        wait_vm<9>();
        bar();
#pragma unroll
        for (int n = 0; n < 10; ++n) {
            if (n == 7) { if (wv * 64 + lane < 200) dma16(P.wphi + g * 3200 + wv * 1024, lane * 16, lds0 + A_WPHI + wv * 1024); }
            if (n < 8) stage(n + 2);
#pragma unroll
            for (int ks = 0; ks < 4; ++ks) xk[n * 4 + ks] = lds_r128(xa[ks] + (n & 3) * A_SLOT);
            if (n < 8) wait_vm<9>();
            else if (n == 8) {
#pragma unroll
                for (int j = 0; j < 5; ++j) {
                    const int rho = ((j * 64 + lane) * 6554) >> 16;
                    wo5[j] = ((rho & 24) + ((rho & 1) << 2) + ((rho >> 1) & 3)) * 3840 - rho * 160 + lane * 16 + 1024;
                }
            }
            if (n >= 8) { stage_w(wq_src((n - 8) * 4), lds0 + wq_pos((n - 8) * 4) + wv * 5120); wait_vm<5>(); }
            bar();
        }
    }
    const int ob = WS_O + ((r * 4 + wv) * 8 + g) * 5120 + lane * 16;
    f32x16 acc[3] = {};
    u32x4 wp[11];
    {
        u32x2 wlo[3][3], whi[3][3];
        lds_ptr qa[2], qb[2];
#pragma unroll
        for (int h = 0; h < 2; ++h) {
            const int o = A_WQ + h * 46080 + (8 * hi + 2 * tq) * 160;
            qa[h] = opaque(L + o + (1 + 4 * (tp & 1) + 2 * g1 + (tp >> 1)) * 8);
            qb[h] = opaque(L + o + (9 * (tp & 1) + 10 * (tp >> 1)) * 8);
        }
        auto rd_q = [&](int s, int t) {
            const int o = wq_pos(s >> 1) - A_WQ, h = o >= 46080;
            lds_ptr b = (t < 2 ? qa[h] + t * 80 : qb[h]) + o - h * 46080 + (s & 1) * 2560;
            wlo[s % 3][t] = lds_tr(b); SBAR();
            whi[s % 3][t] = lds_tr(b + 160); SBAR();
        };
#pragma unroll
        for (int t = 0; t < 3; ++t) rd_q(0, t);
        WphiBases wb;
        WphiOps wo;
        static_for<0, 40>([&](auto S) {
            constexpr int s = S.value, la = s < 4 ? 1 : 2;
            if constexpr (s == 4) {
                wait_vm<0>();
                grp_post(L + C_SLOT, lane);
#pragma unroll
                for (int h = 2; h < 4; ++h) stage_w(wq_src(4 * h), lds0 + wq_pos(4 * h) + wv * 5120);
                grp_wait<1>(L + C_SLOT, 4);
                stage_w(wq_src(16), lds0 + wq_pos(16) + wv * 5120);
                if (wv == 0) {
                    const u64 epoch = ld_agent(P.ws + WS_EPOCH_A) + 1;
                    if (!__all(ld_agent(P.ws + WS_FLAGP + (2 * g + (lane & 1)) * 128) == epoch)) wait_flags(P.ws + WS_FLAGP + 2 * g * 128, 2, lane, epoch);
                    lds_w64(L + A_EPOCH, u32x2{(unsigned)epoch, (unsigned)(epoch >> 32)});
                }
                wb = wphi_bases(L, lane, 1 - hi, 4 * hi);
#pragma unroll
                for (int t = 0; t < 3; ++t) rd_q(5, t);
                wait_vm<0>();
                __builtin_amdgcn_s_setprio(1);
                bar();
            }
            if constexpr (s == 20) grp_post(L + C_DEAD, lane);
            if constexpr (s > 4 && s < 37) __builtin_amdgcn_s_waitcnt(0xC67F);
            SBAR();
#pragma unroll
            for (int t = 0; t < 3; ++t) {
                acc[t] = mfma_w(cat(wlo[s % 3][t], whi[s % 3][t]), xk[s], acc[t]);
                SBAR();
                if constexpr (s + la < 40) rd_q(s + la, t);
                if constexpr (s == 36 || s == 37) { wo.read((s - 36) * 3 + t, wb); SBAR(); }
                if constexpr (s >= 38) {
#pragma unroll
                    for (int e = 6 + ((s - 38) * 3 + t) * 3; e < 9 + ((s - 38) * 3 + t) * 3; ++e) if (e < 20) wo.read(e, wb);
                    SBAR();
                }
            }
        });
        wo.form(wp);
    }
    Projection pr;
    lds_ptr ka[2], kb[2];
    {
        auto quad = [&](int t, int e) { return u32x4{pk(acc[t][e], acc[t][e + 1]), pk(acc[t][e + 2], acc[t][e + 3]), pk(acc[t][e + 4], acc[t][e + 5]), pk(acc[t][e + 6], acc[t][e + 7])}; };
        const bool on = hi == ((n32 >> 2) & 1);
        const int gm = n32 >> 3, ii = n32 & 3;
        pr.xf[0] = quad(0, 0);
#pragma unroll
        for (int i = 0; i < 6; ++i) {
            SBAR();
            pr.product(i, wp);
            SBAR();
            if (i == 0) pr.xf[1] = quad(0, 8);
            if (i == 1) pr.xf[4] = quad(2, 0);
            if (i == 2) pr.xf[2] = quad(1, 0);
            if (i == 3) pr.xf[3] = quad(1, 8);
            if (i >= 4) {
                const int u = i - 4;
                ka[u] = opaque(L + (on ? A_V + (2 * u + hi) * 6144 + (4 * gm + ii) * 80 : A_ZERO));
                kb[u] = opaque(L + (on && gm < 2 ? A_V + (2 * u + hi) * 6144 + (16 + 4 * gm + ii) * 80 : A_ZERO));
            }
        }
        SBAR();
    }
    Range rg;
    rg.all(pr.a[0], pr.a[2], 0);
    static_for<0, 2>([&](auto U) {
        constexpr int u = U.value;
        unsigned F[20];
        u32x4 kf[10];
        f32x16 n[2] = {};
        auto rd = [&](int e) { kf[e] = lds_r128((e < 5 ? kb[u] : ka[u]) + (e % 5) * 16); };
        auto mul = [&](int e) { const int f = e % 5; n[e / 5] = mfma_w(kf[e], u32x4{F[4 * f], F[4 * f + 1], F[4 * f + 2], F[4 * f + 3]}, n[e / 5]); };
        float sum = dual_softmax<true>(pr.a[u], pr.a[2 + u], 4 * u, rg, F,
                                       [&](int i) { if (i) { if (u == 0) pr.product(5 + i, wp); else { rd(i - 1); rd(i + 4); } } },
                                       [&](int c) { if (u == 1) { mul(c); mul(5 + c); } });
        float rr;
        auto put_rem = [&]() {
            __builtin_amdgcn_raw_buffer_store_b64(u32x2{pkn(n[0][0] * rr, n[0][1] * rr), pkn(n[0][2] * rr, n[0][3] * rr)}, wsr, ob - lane * 8 + u * 2560 + 2048, 0, 16);
        };
        if constexpr (u == 0) {
#pragma unroll
            for (int e = 0; e < 20; ++e) asm volatile("" : "+v"(F[e]));
            asm volatile("" : "+v"(pr.a[1]), "+v"(pr.a[3]), "+v"(sum));
            grp_wait<0>(L + C_KV, 4);
#pragma unroll
            for (int e = 0; e < 10; ++e) rd(e);
            SBAR();
#pragma unroll
            for (int e = 0; e < 10; ++e) {
                mul(e);
                if (e < 5) rg.step(e, pr.a[1], pr.a[3], 4);
                if (e == 7) rr = __builtin_amdgcn_rcpf(__builtin_fmaf(sum, 1e-6f, n[0][4]));
                if (e == 8) put_rem();
                SBAR();
            }
        } else {
            rr = __builtin_amdgcn_rcpf(__builtin_fmaf(sum, 1e-6f, n[0][4]));
            put_rem();
            SBAR();
        }
#pragma unroll
        for (int c = 0; c < 2; ++c) {
            const f32x16& a = n[1];
            const int e = c * 8;
            st_ws(wsr, ob + u * 2560 + c * 1024, u32x4{pkn(a[e] * rr, a[e + 1] * rr), pkn(a[e + 2] * rr, a[e + 3] * rr),
                                                       pkn(a[e + 4] * rr, a[e + 5] * rr), pkn(a[e + 6] * rr, a[e + 7] * rr)});
            SBAR();
        }
        if constexpr (u == 0) { grp_post(L + C_O, lane); }
    });
    grp_post(L + C_END, lane);
    if (blockIdx.x == 0 && wv == 0) {
        const u32x2 ev = lds_r64(L + A_EPOCH);
        const u64 epoch = ((u64)ev.y << 32) | ev.x;
        wait_flags(P.ws + WS_FLAGH, 32, lane, epoch);
        grp_wait<8>(L + C_BUMP, 1);
        if (lane == 0) st_agent(P.ws + WS_EPOCH_A, epoch);
    }
}

__host__ __device__ constexpr bool wide(int xcd) { return ((xcd ^ (xcd >> 1)) & 1) == 0; }
__host__ __device__ constexpr int nb_of(int xcd) { return wide(xcd) ? 12 : 8; }
__host__ __device__ constexpr int pf_of(int xcd) { return wide(xcd) ? 8 : 7; }
__device__ __forceinline__ void touch_kv(const Params& P, unsigned dst, int xcd, int q, int lane) {
    const int kb = (q * 3641) >> 16, s = q - kb * 18;
    const bool isx = s < 8;
    const int off = isx ? ((xcd >> 1) * 512 + s * 64) * 1280 + kb * 128
                        : kb * (64 * 3840) + 1280 + (xcd & 1) * 640 + (s - 8) * 128 + (s >= 13 ? 640 : 0);
    touch((isx ? P.x : P.wqkv) + off, lane * (isx ? 1280 : 3840), dst);
}

__device__ __forceinline__ void touch_panel(const char* base, int stride, unsigned dst, int e, int lane) {
    const int kb = (e * 13108) >> 16;
    touch(base + kb * 64 * stride + (e - kb * 5) * 128, lane * stride, dst);
}

__device__ __forceinline__ void role_h(const Params& P, lds_ptr L, int xcd, int i, int ct, int wv) {
    const int lane = threadIdx.x & 63;
    const unsigned lds0 = (unsigned)(uintptr_t)L, dst = lds0 + LDS_BYTES - 256;
    const int nh = 16 - nb_of(xcd), id = wv * nh + 31 - i, n = i & 3, j = (xcd >> 1) * 4 + n;
    bar_raw();
    if (id < 2 && lane < 50) touch(P.wphi + (xcd & 1) * 12800 + id * 6400, lane * 128, dst);
    wait_vm<0>();
    bar_raw();
    if (wide(xcd)) __builtin_amdgcn_s_sleep(KV_PAUSE_W); else __builtin_amdgcn_s_sleep(KV_PAUSE_N);
    for (int q = pf_of(xcd) * nb_of(xcd) + 8 * nh + id; q - id < 230; q += 8 * nh) {
        if (q < 180) touch_kv(P, dst, xcd, q, lane);
        else if (q < 230) touch_panel(P.wqkv + (xcd & 1) * 640, 3840, dst, q - 180, lane);
        bar_raw();
    }
    if (i < 28) {
        const __amdgpu_buffer_rsrc_t wsr = __builtin_amdgcn_make_buffer_rsrc((void*)P.ws, 0, WS_USED, 0x00020000);
        const int t16 = threadIdx.x * 16;
        const u32x4 e4 = {EMPTY, EMPTY, EMPTY, EMPTY};
        const u64 last = ld_agent(P.ws + WS_EPOCH_A);
        for (int m = 0; m < 7; ++m) st_ws(wsr, WS_PART + j * 32 * PART_STRIDE + m * 8192 + t16, e4);
        if (t16 < 2 * PART_STRIDE) st_ws(wsr, WS_RED + j * 2 * PART_STRIDE + t16, e4);
        wait_vm<0>();
        bar_raw();
        if (threadIdx.x == 0) st_agent(P.ws + WS_FLAGP + j * 128, last + 1);
        for (int m = 0; m < 20; ++m) st_ws(wsr, WS_O + j * 163840 + m * 8192 + t16, e4);
        wait_vm<0>();
        bar_raw();
        if (threadIdx.x == 0) st_agent(P.ws + WS_FLAGO + j * 128, last + 1);
        return;
    }
    wait_vm<0>();
    __builtin_amdgcn_s_sleep(8);
    for (int e = wv * 4 + n; e < 64; e += 32) {
        if (e < 50) touch_panel(P.wo + (xcd & 1) * 640, 1280, dst, e, lane);
        bar_raw();
    }
    const u64 epoch = ld_agent(P.ws + WS_EPOCH_A) + 1;
    stage_wo(P, lds0, ct, 1, wv, wo_voff(lane));
    wait_vm<0>();
    bar();
    if (threadIdx.x == 0) st_agent(P.ws + WS_FLAGH + (xcd * 4 + n) * 128, epoch);
    if (wv == 0) wait_flags(P.ws + WS_FLAGU + (j * 2 + (xcd & 1)) * 128, 1, lane, epoch);
    bar_raw();
}

constexpr int B_HS = 8512, B_H1 = 64, B_H0 = 64 + 4096 + 128;
constexpr int B_R = 8 * B_HS, B_RS = 9152, B_R0 = 4576;
constexpr int B_K = 416, B_V = B_K + 8 * 1056;
constexpr int B_CTR = LDS_BYTES - 512;
constexpr int B_Y = B_R + 8 * B_RS, B_YS = 2112;

__device__ __forceinline__ void role_b(const Params& P, lds_ptr L, int xcd, int n, int wv) {
    const int tid = threadIdx.x, lane = tid & 63;
    const int col = n < 8 ? (xcd == 2 ? 9 : xcd) : (xcd < 4 ? 2 : 8), j8 = n < 8 ? n : (xcd & 1) * 4 + n - 8, grp = col * 8 + j8;
    const unsigned lds0 = (unsigned)(uintptr_t)L;
    if (tid < 4) lds_w32(L + B_CTR + tid * 4, 0u);
    bar();

    lds_ptr R1 = L + B_R + wv * B_RS, RP = L + B_R + (wv & 6) * B_RS;
    {
        const unsigned so = (lane >> 3) * 1280 + (lane & 7) * 16, d0 = lds0 + B_R + (wv & 6) * B_RS;
        const int tb = (j8 * 256 + (wv & 6) * 32) * 1280 + col * 128;
        if (!(wv & 1) && lane >= 40) {
            if (j8 + wv > 0) dma16(P.k + tb - 8 * 1280, so, d0 + B_K - 1056);
            else lds_w128(RP + B_K - 1056 + lane * 16, u32x4{0, 0, 0, 0});
        }
        const char* src = ((wv & 1) ? P.v : P.k) + tb;
#pragma unroll
        for (int h = 0; h < 2; ++h) {
            set_m0(d0 + ((wv & 1) ? B_V : B_K) + h * 4 * 1056);
#pragma unroll
            for (int m = 0; m < 4; ++m) dma_once(m * 1056, src + h * 40960 + m * (10240 - 1056), so);
        }
    }
    const int ch = grp * 8 + wv;
    const int tau = 7 - (lane & 7), cq = (wv & 1) * 8 + (lane >> 3);
    u32x4 sw0, sw1; u64 epoch; unsigned dval;
    asm volatile("global_load_dwordx4 %0, %4, %5\n\tglobal_load_dwordx4 %1, %4, %5 offset:16\n\t"
                 "global_load_dwordx2 %2, %6, %7 sc1\n\tglobal_load_ushort %3, %6, %8"
                 : "=&v"(sw0), "=&v"(sw1), "=&v"(epoch), "=&v"(dval)
                 : "v"(cq * 32), "s"(P.shk + col * 512), "v"(0), "s"(P.ws + WS_EPOCH_B + col * 128), "s"(P.dv + ch * 2) : "memory");
    const __amdgpu_buffer_rsrc_t wsr = __builtin_amdgcn_make_buffer_rsrc((void*)P.ws, 0, WS_USED, 0x00020000);
    const int uo = WS_U + (col * 64 + cq * 4) * U_STRIDE + (255 - (j8 * 32 + (wv & 6) * 4 + tau)) * 16;
    if (lane < 16) { lds_w32(L + wv * B_HS + lane * 4, 0u); lds_w32(L + wv * B_HS + B_H0 - 64 + lane * 4, 0u); }
    asm volatile("s_waitcnt vmcnt(0)" : "+v"(sw0), "+v"(sw1), "+v"(epoch), "+v"(dval) :: "memory");
    grp_post(L + B_CTR + (wv & 6) * 2, lane);
    epoch += 1;
    grp_wait<1>(L + B_CTR + (wv & 6) * 2, 2);
    {
        lds_ptr kp = opaque(RP + tau * 1056 + cq * 8);
        u32x2 kr[11], vr[8];
#pragma unroll
        for (int e = 0; e < 11; ++e) { kr[e] = lds_r64(kp + (e < 3 ? e * 128 : B_K + (e - 3) * 128)); SBAR(); }
#pragma unroll
        for (int e = 0; e < 8; ++e) { vr[e] = lds_r64(kp + B_V + e * 128); SBAR(); }
        f32x2 w[2][4];
        w[0][0] = f32x2{blo(sw0.x), blo(sw0.z)}; w[0][1] = f32x2{bhi(sw0.x), bhi(sw0.z)};
        w[0][2] = f32x2{blo(sw0.y), blo(sw0.w)}; w[0][3] = f32x2{bhi(sw0.y), bhi(sw0.w)};
        w[1][0] = f32x2{blo(sw1.x), blo(sw1.z)}; w[1][1] = f32x2{bhi(sw1.x), bhi(sw1.z)};
        w[1][2] = f32x2{blo(sw1.y), blo(sw1.w)}; w[1][3] = f32x2{bhi(sw1.y), bhi(sw1.w)};
#pragma unroll
        for (int h = 0; h < 2; ++h) {
            f32x2 kk[11], u[8];
#pragma unroll
            for (int e = 0; e < 11; ++e) kk[e] = f32x2{blo(kr[e][h]), bhi(kr[e][h])};
#pragma unroll
            for (int e = 0; e < 8; ++e) u[e] = kk[e] * w[h][3];
#pragma unroll
            for (int e = 0; e < 8; ++e) u[e] = __builtin_elementwise_fma(kk[e + 1], w[h][2], u[e]);
#pragma unroll
            for (int e = 0; e < 8; ++e) u[e] = __builtin_elementwise_fma(kk[e + 2], w[h][1], u[e]);
#pragma unroll
            for (int e = 0; e < 8; ++e) u[e] = __builtin_elementwise_fma(kk[e + 3], w[h][0], u[e]);
#pragma unroll
            for (int e = 0; e < 8; ++e) u[e] *= f32x2{blo(vr[e][h]), bhi(vr[e][h])};
#pragma unroll
            for (int e = 0; e < 2; ++e)
                st_ws(wsr, uo + (2 * h + e) * U_STRIDE, u32x4{pk(u[7][e], u[6][e]), pk(u[5][e], u[4][e]), pk(u[3][e], u[2][e]), pk(u[1][e], u[0][e])});
            SBAR();
        }
    }
    auto second_h = [&]() {
        lds_ptr hp = opaque(L + wv * B_HS + B_H1 - 4 + lane * 16);
        u32x4 a[4]; unsigned pv[4];
#pragma unroll
        for (int m = 0; m < 4; ++m) { a[m] = lds_r128(hp + 4 + m * 1024); pv[m] = lds_r32(hp + m * 1024); }
#pragma unroll
        for (int m = 0; m < 4; ++m)
            lds_w128(hp + 4 + (B_H0 - B_H1) + m * 1024, u32x4{__builtin_amdgcn_alignbit(a[m].x, pv[m], 16), __builtin_amdgcn_alignbit(a[m].y, a[m].x, 16),
                                                              __builtin_amdgcn_alignbit(a[m].z, a[m].y, 16), __builtin_amdgcn_alignbit(a[m].w, a[m].z, 16)});
    };
    bar_raw();
    set_m0(lds0 + wv * B_HS + B_H1);
#pragma unroll
    for (int m = 0; m < 2; ++m) dma_once(m * 1024, P.hk + ch * 4096, lane * 16);
    wait_vm<2>();
    bar_raw();
    if (tid == 0) st_agent(P.ws + WS_FLAGB + grp * 128, epoch);
    if (lane < 28) { lds_w128(R1 + 4096 + lane * 16, u32x4{0, 0, 0, 0}); lds_w128(R1 + B_R0 + 4096 + lane * 16, u32x4{0, 0, 0, 0}); }
    else if (lane == 28) lds_w32(R1 + B_R0 + 4092, 0u);
    set_m0(lds0 + wv * B_HS + B_H1);
#pragma unroll
    for (int m = 2; m < 4; ++m) dma_once(m * 1024, P.hk + ch * 4096, lane * 16);
    wait_vm<0>();
    if (wv != 0) second_h();
    else {
        wait_flags(P.ws + WS_FLAGB + col * 8 * 128, 8, lane, epoch);
        if (j8 == 0 && lane == 0) st_agent(P.ws + WS_EPOCH_B + col * 128, epoch);
    }
    bar_raw();
#pragma unroll
    for (int h = 0; h < 2; ++h) {
        set_m0(lds0 + B_R + wv * B_RS + (h ? B_R0 - 4 : 0));
#pragma unroll
        for (int m = 0; m < 4; ++m)
            asm volatile("global_load_lds_dwordx4 %0, %1 offset:%2 sc1" :: "v"(lane * 16), "s"(P.ws + WS_U + ch * U_STRIDE), "i"(m * 1024) : "memory");
    }
    if (wv == 0) second_h();

    const int c16 = lane & 15, kq = lane >> 4;
    lds_ptr ub = opaque(R1 + ((c16 & 1) ? 0 : B_R0) + 448 - 64 * (c16 >> 1) + 16 * kq);
    lds_ptr hb = opaque(L + wv * B_HS + ((c16 & 1) ? B_H1 : B_H0) + 2 * (4 * (c16 >> 1) - 28 + 8 * kq));
    lds_ptr hb2 = opaque(hb + 8);
    const int yo = 2 * (wv * 64 + kq * 16 + (lane & 3) * 4), ypo = 4 * (c16 & 1) + 64 * (c16 >> 1) + 16 * kq;
    lds_ptr yr0 = opaque(L + (c16 >> 2) * B_HS + B_H1 + 1536 + yo), yr1 = opaque(L + B_Y + (c16 >> 2) * B_YS + yo);
    lds_ptr yp0 = opaque(L + wv * B_HS + B_H1 + 1536 + ypo), yp1 = opaque(L + B_Y + wv * B_YS + ypo);
    f32x4 y[8];
    u32x4 qv[4], U[2][8];
    u32x2 Ha[2][8], Hb[2][8];
    const int he = 28 - 4 * (c16 >> 1) - 8 * kq;
    const unsigned dsh = (c16 & 1) ? dval : dval << 16;
    const u32x4 hd = {he == 0 ? dsh : 0u, 0u, he == 4 ? dsh : 0u, 0u};
    auto rd_c = [&](int m, int e) {
        const int rr = m & 7, k = e / 3, s = m < 8 ? k : (k & 1) + 6 * (k >> 1), i = m < 8 ? k : (k & 1) + 6 * (1 - (k >> 1));
        if (e % 3 == 0) U[m & 1][i] = lds_r128(ub + 512 * (7 - i) + 64 * rr);
        else if (e % 3 == 1) Ha[m & 1][s] = lds_r64(hb + 512 * s + 64 * rr);
        else Hb[m & 1][s] = lds_r64(hb2 + 512 * s + 64 * rr);
        SBAR();
    };
    auto put_y = [&](int j) {
        lds_ptr p = j < 4 ? yp0 + (j >> 1) * (B_H0 - B_H1) + (j & 1) * 512 : yp1 + (j - 4) * 512;
        lds_w32(p, pk(y[j][0], y[j][1])); lds_w32(p + 8, pk(y[j][2], y[j][3]));
    };
    auto emit = [&](int m) {
        const u32x4 yy = m < 2 ? cat(lds_tr(yr0 + m * (B_H0 - B_H1)), lds_tr(yr0 + m * (B_H0 - B_H1) + 4 * B_HS))
                               : cat(lds_tr(yr1 + (m - 2) * 1024), lds_tr(yr1 + (m - 2) * 1024 + 4 * B_YS));
        u32x4 v;
#pragma unroll
        for (int e = 0; e < 4; ++e) v[e] = pk(blo(yy[e]) * blo(qv[m][e]), bhi(yy[e]) * bhi(qv[m][e]));
        *(u32x4*)(P.out + (size_t)(m * 512 + tid) * 2560 + 1280 + grp * 16) = v;
    };
    if (wv < 4) __builtin_amdgcn_s_setprio(2); else __builtin_amdgcn_s_setprio(1);
    wait_vm<0>();
#pragma unroll
    for (int e = 0; e < 18; ++e) rd_c(0, e);
    y[6] = f32x4{0.f, 0.f, 0.f, 0.f}; y[7] = y[6];
    static_for<0, 16>([&](auto M) {
        constexpr int m = M.value, nrd = m == 15 ? 0 : (m < 7 ? 18 : 12);
        if constexpr (m == 1) {
            if (n >= 8 || wv < 4) touch(P.q + (n < 8 ? wv * 8 + n : wv * 4 + n - 8) * (64 * 1280) + col * 128, lane * 1280, lds0 + LDS_BYTES - 256);
        }
        if constexpr (m >= 4 && m < 8) qv[m - 4] = *(const u32x4*)(P.q + (size_t)((m - 4) * 512 + tid) * 1280 + grp * 16);
        if constexpr (m == 7) { if (wv < 4) __builtin_amdgcn_s_setprio(0); }
        if constexpr (m > 0 && m < 8) __builtin_amdgcn_s_waitcnt(0xC07F);
        if constexpr (m == 0) {
#pragma unroll
            for (int j = 0; j < 6; ++j) y[j] = mfma32(hd, U[0][j], f32x4{0.f, 0.f, 0.f, 0.f});
        }
        if constexpr (m == 8) {
            y[6] = mfma32(hd, U[0][6], y[6]); y[7] = mfma32(hd, U[0][7], y[7]);
#pragma unroll
            for (int j = 0; j < 6; ++j) put_y(j);
            bar();
            emit(0);
        }
        if constexpr (m == 9) emit(1);
        if constexpr (m == 10) emit(2);
        SBAR();
        int done = 0;
#pragma unroll
        for (int s = 0; s < 8; ++s)
#pragma unroll
            for (int i = 0; i + s < 8; ++i) {
                if ((s < 6 && i < 6) != (m < 8)) continue;
                y[s + i] = mfma32(cat(Ha[m & 1][s], Hb[m & 1][s]), U[m & 1][i], y[s + i]);
                SBAR();
#pragma unroll
                for (int e = 0; e < (m < 8 ? 1 : 2); ++e) if (done < nrd) { rd_c(m + 1, done); ++done; }
            }
    });
    put_y(6); put_y(7);
    bar();
    emit(3);
    END_WAVE();
}

__global__ void __launch_bounds__(512, 2) fused_kernel(Params P) {
    __shared__ __attribute__((aligned(16))) char smem[LDS_BYTES];
    lds_ptr L = (lds_ptr)smem;
    unsigned ka0, ka1;
    asm volatile("s_load_dword %0, %2, 0x0\n\ts_load_dword %1, %2, 0x40" : "=&s"(ka0), "=&s"(ka1) : "s"(__builtin_amdgcn_kernarg_segment_ptr()));
    const int xcd = blockIdx.x & 7, i = blockIdx.x >> 3;
    const int wv = __builtin_amdgcn_readfirstlane(threadIdx.x >> 6);
    if (wv == 7) {
        const char* pc; unsigned len;
        asm volatile("s_getpc_b64 %0\nhk_pc:\n\ts_mov_b32 %1, hk_code_end - hk_pc\n\t.subsection 1\nhk_code_end:\n\t.subsection 0" : "=s"(pc), "=s"(len));
        const unsigned o = ((threadIdx.x & 63) * 32 + i) * 128;
        if (o < len && o < 49152) touch(pc, o, (unsigned)(uintptr_t)L + LDS_BYTES - 256);
    }
    const bool is_a = i < 16;
    const int r = (xcd >> 1) * 4 + (i & 3), g = (xcd & 1) * 4 + (i >> 2);
    const int ct = (xcd & 1) * 5 + (is_a ? i >> 2 : 4), rot = is_a ? 2 * (i >> 2) : 1;
    {
        const int lane = threadIdx.x & 63;
        const unsigned lds0 = (unsigned)(uintptr_t)L;
        if (is_a) {
            if (wv < 4) {
                int off = (r * 128 + wv * 32) * 1280;
                unsigned v[4];
#pragma unroll
                for (int j = 0; j < 4; ++j) v[j] = (lane >> 3) * 1280 + (((lane & 7) ^ (4 * (j & 1) + (lane >> 4))) * 16) + j * (8 * 1280 - 1024);
                set_m0(lds0 + A_X + wv * 4096);
                asm volatile("" : "+v"(v[0]), "+v"(v[1]), "+v"(v[2]), "+v"(v[3]), "+s"(off));
#pragma unroll
                for (int j = 0; j < 4; ++j) dma_off(j * 1024, P.x + (unsigned)off, v[j]);
            } else {
                int off = 1280 * (1 + (wv & 1)) + g * 160 + ((wv >> 1) & 1) * (32 * 3840);
                unsigned v[5];
#pragma unroll
                for (int j = 0; j < 5; ++j) v[j] = (((j * 64 + lane) * 6554) >> 16) * (3840 - 160) + lane * 16 + 1024;
                set_m0(lds0 + 1024 + (wv & 1) * A_WV + ((wv >> 1) & 1) * 5120);
                asm volatile("" : "+v"(v[0]), "+v"(v[1]), "+v"(v[2]), "+v"(v[3]), "+v"(v[4]), "+s"(off));
#pragma unroll
                for (int j = 0; j < 5; ++j) dma_off((j - 1) * 1024, P.wqkv + (unsigned)off, v[j]);
            }
        } else if (i >= 16 + nb_of(xcd) || wv < pf_of(xcd)) {
            touch_kv(P, lds0 + LDS_BYTES - 256, xcd, wv < pf_of(xcd) ? wv * 16 + i - 16 : pf_of(xcd) * nb_of(xcd) + wv * (16 - nb_of(xcd)) + 31 - i, lane);
        }
        asm volatile("s_waitcnt lgkmcnt(0)" :: "s"(ka0), "s"(ka1));
    }
    int i2 = i, w2 = wv;
    asm volatile("" : "+s"(i2), "+s"(w2));
    bool out = false;
    if (i2 < 16 && w2 < 4) { role_k(P, L, r, g, ct, rot, wv); out = true; }
    else if (i2 < 16) role_q(P, L, r, g, wv - 4);
    else if (i2 < 16 + nb_of(xcd)) role_b(P, L, xcd, i - 16, wv);
    else { role_h(P, L, xcd, i, ct, wv); out = i >= 28 && wv < 4; }
    if (out) {
        out_tile(P, L, r, ct, rot, wv, threadIdx.x & 63);
    }
}

}

extern "C" void kernel_launch(void* const* d_in, const int* in_sizes, int n_in,
                              void* d_out, int out_size, void* d_ws, size_t ws_size,
                              hipStream_t stream) {
    hk::Params P;
    P.x = (const char*)d_in[0]; P.wqkv = (const char*)d_in[1]; P.wphi = (const char*)d_in[2]; P.wo = (const char*)d_in[3];
    P.q = (const char*)d_in[4]; P.k = (const char*)d_in[5]; P.v = (const char*)d_in[6];
    P.shk = (const char*)d_in[7]; P.hk = (const char*)d_in[8]; P.dv = (const char*)d_in[9];
    P.out = (char*)d_out; P.ws = (char*)d_ws;
    hk::fused_kernel<<<256, 512, 0, stream>>>(P);
}
```
